```python
import torch

def _softmax(x, axis=-1):
    return torch.softmax(x, dim=axis)
def _gelu(x):
    return torch.nn.functional.gelu(x, approximate="tanh")
def _silu(x):
    return torch.nn.functional.silu(x)
def _sigmoid(x):
    return torch.sigmoid(x)
def _layernorm(x, axis=-1, eps=1e-5):
    m = x.mean(dim=axis, keepdim=True)
    v = x.var(dim=axis, keepdim=True, unbiased=False)
    return (x - m) / torch.sqrt(v + eps)
def _scatter_add(tgt, idx, src, axis=0):
    return tgt.clone().index_add_(axis, idx, src)
def _segment_sum(src, seg, n):
    out = torch.zeros((n,) + src.shape[1:], dtype=src.dtype)
    return out.index_add_(0, seg, src)
def _pad(x, pw):
    flat = tuple(v for pair in reversed(pw) for v in pair)
    return torch.nn.functional.pad(x, flat)

kernel_name = 'concat_heads__mha_block_cached__gqa_block_cached__near_ridge'

def setup_inputs():

    _w = (lambda a: (lambda t: t.to(torch.bfloat16) if t.is_floating_point() else t)(torch.as_tensor(a)))
    def _A_setup():
        B, Sq, D = 8, 144, 576
        H, Sk, Dh = 32, 512, 128
        return {
            "x": _w((lambda *s: torch.randn(*s, dtype=torch.bfloat16) * 0.8)(B, Sq, D)),
            "k_cache": _w((lambda *s: torch.randn(*s, dtype=torch.bfloat16) * 0.8)(B, H, Sk, Dh)),
            "v_cache": _w((lambda *s: torch.randn(*s, dtype=torch.bfloat16) * 0.05)(B, H, Sk, Dh)),
            "wq": _w((lambda *s: torch.randn(*s, dtype=torch.bfloat16) * 0.8)(D, H * Dh) * (D ** -0.5)),
            "wo": _w((lambda *s: torch.randn(*s, dtype=torch.bfloat16) * 0.05)(H * Dh, D) * ((H * Dh) ** -0.5)),
        }
    _A_in = _A_setup()

    def _B_setup():
        B, Sq, D = 8, 144, 576
        Hq, Hkv, Sk, Dh = 32, 32, 512, 128
        assert Hq % Hkv == 0, (Hq, Hkv)
        return {
            "x": _w((lambda *s: torch.randn(*s, dtype=torch.bfloat16) * 0.8)(B, Sq, D)),
            "k_cache": _w((lambda *s: torch.randn(*s, dtype=torch.bfloat16) * 0.8)(B, Hkv, Sk, Dh)),
            "v_cache": _w((lambda *s: torch.randn(*s, dtype=torch.bfloat16) * 0.05)(B, Hkv, Sk, Dh)),
            "wq": _w((lambda *s: torch.randn(*s, dtype=torch.bfloat16) * 0.8)(D, Hq * Dh) * (D ** -0.5)),
            "wo": _w((lambda *s: torch.randn(*s, dtype=torch.bfloat16) * 0.05)(Hq * Dh, D) * ((Hq * Dh) ** -0.5)),
        }
    _B_in = _B_setup()

    return {
        "A_x": _A_in["x"],
        "A_k_cache": _A_in["k_cache"],
        "A_v_cache": _A_in["v_cache"],
        "A_wq": _A_in["wq"],
        "A_wo": _A_in["wo"],
        "B_x": _B_in["x"],
        "B_k_cache": _B_in["k_cache"],
        "B_v_cache": _B_in["v_cache"],
        "B_wq": _B_in["wq"],
        "B_wo": _B_in["wo"],
    }

def reference(A_x, A_k_cache, A_v_cache, A_wq, A_wo, B_x, B_k_cache, B_v_cache, B_wq, B_wo):
    def _A_ref(x, k_cache, v_cache, wq, wo):
        B, Sq, D = x.shape
        H, Dh = 32, 128
        q = (lambda x, *d: x.permute(*d))((x @ wq).reshape(B, Sq, H, Dh), 0, 2, 1, 3)
        scores = torch.einsum("bhqd,bhkd->bhqk", q, k_cache) * (Dh ** -0.5)
        p = _softmax(scores, axis=-1)
        o = torch.einsum("bhqk,bhkd->bhqd", p, v_cache)
        return (lambda x, *d: x.permute(*d))(o, 0, 2, 1, 3).reshape(B, Sq, H * Dh) @ wo

    def _B_ref(x, k_cache, v_cache, wq, wo):
        B, Sq, D = x.shape
        Hq, Dh = 32, 128
        Hkv = k_cache.shape[1]
        rep = Hq // Hkv
        q = (lambda x, *d: x.permute(*d))((x @ wq).reshape(B, Sq, Hq, Dh), 0, 2, 1, 3)
        k = (lambda x, n, axis: torch.repeat_interleave(x, n, dim=axis))(k_cache, rep, axis=1)
        v = (lambda x, n, axis: torch.repeat_interleave(x, n, dim=axis))(v_cache, rep, axis=1)
        scores = torch.einsum("bhqd,bhkd->bhqk", q, k) * (Dh ** -0.5)
        p = _softmax(scores, axis=-1)
        o = torch.einsum("bhqk,bhkd->bhqd", p, v)
        return (lambda x, *d: x.permute(*d))(o, 0, 2, 1, 3).reshape(B, Sq, Hq * Dh) @ wo

    a = _A_ref(A_x, A_k_cache, A_v_cache, A_wq, A_wo)
    b = _B_ref(B_x, B_k_cache, B_v_cache, B_wq, B_wo)
    return torch.concatenate([a, b], axis=-1)
```

```cpp
#include <hip/hip_runtime.h>
#include <hip/hip_bf16.h>
#include <stdint.h>

typedef __attribute__((ext_vector_type(8))) __bf16 bf16x8;
typedef __attribute__((ext_vector_type(4))) __bf16 bf16x4;
typedef __attribute__((ext_vector_type(2))) __bf16 bf16x2;
typedef __attribute__((ext_vector_type(4))) float f32x4;
typedef __attribute__((ext_vector_type(2))) float f32x2;
typedef __attribute__((ext_vector_type(4))) unsigned u32x4;
typedef __attribute__((ext_vector_type(2))) unsigned u32x2;
#define LDS_AS __attribute__((address_space(3)))
typedef __amdgpu_buffer_rsrc_t rsrc_t;

constexpr int NB = 8, SQ = 144, DM = 576, NH = 32, SK = 512, DH = 128, HD = NH * DH;

#define STAMPAT(kern, idx)
#define STAMP_ARG
#define STAMP_PASS

__device__ __forceinline__ rsrc_t mk_rsrc(const void* p, int bytes) {
  return __builtin_amdgcn_make_buffer_rsrc((void*)p, (short)0, bytes, 0x00020000);
}
template <int NT>
__device__ __forceinline__ void dma16(rsrc_t r, unsigned voff, unsigned soff, unsigned ldsb) {
  if constexpr (NT)
    asm volatile("s_mov_b32 m0, %0\n\ts_nop 0\n\tbuffer_load_dwordx4 %1, %2, %3 offen nt lds" ::"s"(ldsb), "v"(voff), "s"(r), "s"(soff) : "memory");
  else
    asm volatile("s_mov_b32 m0, %0\n\ts_nop 0\n\tbuffer_load_dwordx4 %1, %2, %3 offen lds" ::"s"(ldsb), "v"(voff), "s"(r), "s"(soff) : "memory");
}
template <int N>
__device__ __forceinline__ void wait_bar() {
  asm volatile("s_waitcnt vmcnt(%0) lgkmcnt(0)\n\ts_barrier" ::"i"(N) : "memory");
}
__device__ __forceinline__ bf16x8 lds_b128(unsigned a) { return *(const LDS_AS bf16x8*)(uintptr_t)a; }
__device__ __forceinline__ bf16x4 lds_tr(unsigned a) { return __builtin_amdgcn_ds_read_tr16_b64_v4bf16((LDS_AS bf16x4*)(uintptr_t)a); }
__device__ __forceinline__ bf16x8 lds_tr2(unsigned a0, unsigned a1) {
  bf16x4 x = lds_tr(a0), y = lds_tr(a1);
  return __builtin_shufflevector(x, y, 0, 1, 2, 3, 4, 5, 6, 7);
}
__device__ __forceinline__ f32x4 mfma(bf16x8 a, bf16x8 b, f32x4 c) { return __builtin_amdgcn_mfma_f32_16x16x32_bf16(a, b, c, 0, 0, 0); }
__device__ __forceinline__ unsigned pk2(float a, float b) {
  f32x2 v = {a, b};
  bf16x2 r = __builtin_convertvector(v, bf16x2);
  return __builtin_bit_cast(unsigned, r);
}
__device__ __forceinline__ bf16x8 pack8(f32x4 a, f32x4 b) {
  u32x4 w = {pk2(a[0], a[1]), pk2(a[2], a[3]), pk2(b[0], b[1]), pk2(b[2], b[3])};
  return __builtin_bit_cast(bf16x8, w);
}
__device__ __forceinline__ float gmax(float v) {
  auto a = __builtin_amdgcn_permlane16_swap(__float_as_uint(v), __float_as_uint(v), false, false);
  v = fmaxf(__uint_as_float(a[0]), __uint_as_float(a[1]));
  auto b = __builtin_amdgcn_permlane32_swap(__float_as_uint(v), __float_as_uint(v), false, false);
  return fmaxf(__uint_as_float(b[0]), __uint_as_float(b[1]));
}
__device__ __forceinline__ float gsum(float v) {
  auto a = __builtin_amdgcn_permlane16_swap(__float_as_uint(v), __float_as_uint(v), false, false);
  v = __uint_as_float(a[0]) + __uint_as_float(a[1]);
  auto b = __builtin_amdgcn_permlane32_swap(__float_as_uint(v), __float_as_uint(v), false, false);
  return __uint_as_float(b[0]) + __uint_as_float(b[1]);
}
__device__ __forceinline__ float max4(f32x4 a) { return fmaxf(fmaxf(a[0], a[1]), fmaxf(a[2], a[3])); }

#define NT_KV 1
#define NT_W 0
constexpr float QSCALE = 0.08838834764831845f * 1.4426950408889634f;
constexpr float THR = 8.f;
constexpr int SLOT = 16384, QST0 = 16384, QSTB = 17408, Q8OFF = 77824;

struct Ptrs { const void *x, *k, *v, *wq, *wo; };

__global__ __launch_bounds__(256, 2) void attn_kernel(Ptrs pa, Ptrs pb, u32x4* __restrict__ ows STAMP_ARG) {
  __shared__ __attribute__((aligned(16))) char smem[81920];
  const unsigned lb = (unsigned)(uintptr_t)(LDS_AS char*)smem;
  STAMPAT(0, 0);
  const int lane = threadIdx.x & 63, w = __builtin_amdgcn_readfirstlane(threadIdx.x >> 6);
  const int c16 = lane & 15, g = lane >> 4;
  const int xcd = blockIdx.x & 7, j = blockIdx.x >> 3;
  const int br = xcd >> 2, b = j & 7, hl = j >> 3, h = (xcd & 3) * 8 + hl;
  const Ptrs p = br ? pb : pa;
  const rsrc_t rx = mk_rsrc((const char*)p.x + (size_t)b * SQ * DM * 2, SQ * DM * 2);
  const rsrc_t rw = mk_rsrc((const char*)p.wq + h * DH * 2, DM * HD * 2 - h * DH * 2);
  const rsrc_t rk = mk_rsrc((const char*)p.k + (size_t)(b * NH + h) * SK * DH * 2, SK * DH * 2);
  const rsrc_t rv = mk_rsrc((const char*)p.v + (size_t)(b * NH + h) * SK * DH * 2, SK * DH * 2);

  const unsigned fw = (g & 1) | ((g & 2) << 1) | ((w & 1) << 3);
  const unsigned vo_w = (8 * w + g) * (HD * 2) + ((c16 ^ fw) << 4);
  const unsigned vo_x = (lane >> 2) * (DM * 2) + (((lane & 3) ^ ((lane >> 5) << 1)) << 4);
  const int r8 = (lane >> 2) & 7;
  const unsigned vo_kv = (8 * (w >> 1) + r8) * 256 + (2 * (w & 1) + (lane >> 5)) * 64;
  const unsigned vo_k = vo_kv + (((lane & 3) ^ (w & 2)) << 4);
  const unsigned vo_v = vo_kv + (((lane & 3) ^ ((r8 & 4) >> 1)) << 4);

  auto issue_q = [&](int kc, int buf) {
    const unsigned sb = lb + QST0 + buf * QSTB;
    const unsigned sw = kc * 32 * HD * 2, sx = kc * 64;
    dma16<NT_W>(rw, vo_w, sw, sb + w * 2048);
    dma16<NT_W>(rw, vo_w, sw + 4 * HD * 2, sb + w * 2048 + 1024);
    dma16<NT_W>(rx, vo_x, sx + (2 * w) * 16 * DM * 2, sb + 8192 + (2 * w) * 1024);
    dma16<NT_W>(rx, vo_x, sx + (2 * w + 1) * 16 * DM * 2, sb + 8192 + (2 * w + 1) * 1024);
    if (g == w) dma16<NT_W>(rx, vo_x, sx + 8 * 16 * DM * 2, sb + 8192 + 8 * 1024);
  };
  auto issue_kv = [&](int t) {
    const unsigned sb = lb + (t % 5) * SLOT + w * 1024, so = (t >> 1) * SLOT;
#pragma unroll
    for (int i = 0; i < 4; ++i) {
      if (t & 1) dma16<NT_KV>(rv, vo_v, so + i * 4096, sb + i * 4096);
      else dma16<NT_KV>(rk, vo_k, so + i * 4096, sb + i * 4096);
    }
  };

  int kc = 2 * ((b + hl) & 7);
  issue_q(kc, 0); kc = kc + 1;
  issue_q(kc, 1); kc = kc + 1;
  issue_kv(0);
  f32x4 qa[2][8], q8[2];
#pragma unroll
  for (int t = 0; t < 8; ++t) { qa[0][t] = f32x4{0, 0, 0, 0}; qa[1][t] = f32x4{0, 0, 0, 0}; }
  q8[0] = f32x4{0, 0, 0, 0}; q8[1] = f32x4{0, 0, 0, 0};
  unsigned aw[4];
  {
    const int qq = c16 >> 2, pp = c16 & 3;
    const unsigned f = (qq & 1) | ((qq & 2) << 1) | ((g & 1) << 3);
#pragma unroll
    for (int s = 0; s < 4; ++s) aw[s] = lb + QST0 + (8 * g + qq) * 256 + ((((unsigned)(4 * (s ^ w) + pp)) ^ f) << 4) + 8 * (pp & 1);
  }
  const unsigned ax = lb + QST0 + 8192 + c16 * 64 + ((g ^ ((c16 & 8) >> 2)) << 4);
  auto q_stage = [&](int buf) {
    const unsigned o = buf * QSTB;
    const bf16x8 x0 = lds_b128(ax + o + (2 * w) * 1024), x1 = lds_b128(ax + o + (2 * w + 1) * 1024), x8 = lds_b128(ax + o + 8192);
#pragma unroll
    for (int s = 0; s < 4; ++s)
#pragma unroll
      for (int u = 0; u < 2; ++u) {
        const unsigned a = (u ? (aw[s] ^ 8) : aw[s]) + o;
        const bf16x8 wf = lds_tr2(a, a + 1024);
        qa[0][2 * s + u] = mfma(wf, x0, qa[0][2 * s + u]);
        qa[1][2 * s + u] = mfma(wf, x1, qa[1][2 * s + u]);
        if (s == 0) q8[u] = mfma(wf, x8, q8[u]);
      }
  };
  wait_bar<9>(); issue_q(kc, 2); kc = kc == 17 ? 0 : kc + 1; q_stage(0);
  wait_bar<9>(); issue_q(kc, 0); kc = kc == 17 ? 0 : kc + 1; q_stage(1);
  wait_bar<5>(); issue_q(kc, 1); kc = kc == 17 ? 0 : kc + 1; q_stage(2);
  for (int it = 0; it < 4; ++it) {
    wait_bar<5>(); issue_q(kc, 2); kc = kc == 17 ? 0 : kc + 1; q_stage(0);
    wait_bar<5>(); issue_q(kc, 0); kc = kc == 17 ? 0 : kc + 1; q_stage(1);
    wait_bar<5>(); issue_q(kc, 1); kc = kc == 17 ? 0 : kc + 1; q_stage(2);
  }
  wait_bar<5>(); issue_q(kc, 2); q_stage(0);
  wait_bar<5>(); q_stage(1);
  wait_bar<0>(); q_stage(2);
  STAMPAT(0, 1);
  bf16x8 qf[3][4];
  auto qpack = [&](f32x4 lo, f32x4 hi) {
    lo *= QSCALE; hi *= QSCALE;
    const unsigned a0 = pk2(lo[0], lo[1]), a1 = pk2(lo[2], lo[3]), b0 = pk2(hi[0], hi[1]), b1 = pk2(hi[2], hi[3]);
    const bool odd = g & 1;
    u32x4 r = {odd ? b0 : a0, odd ? b1 : a1, odd ? a0 : b0, odd ? a1 : b1};
    return __builtin_bit_cast(bf16x8, r);
  };
  *(LDS_AS bf16x8*)(uintptr_t)(lb + Q8OFF + w * 1024 + lane * 16) = qpack(q8[0], q8[1]);
#pragma unroll
  for (int s = 0; s < 4; ++s) { qf[0][s] = qpack(qa[0][2 * s], qa[0][2 * s + 1]); qf[1][s] = qpack(qa[1][2 * s], qa[1][2 * s + 1]); }
#pragma unroll
  for (int a = 0; a < 2; ++a) {
    if (w & 1) { bf16x8 t = qf[a][0]; qf[a][0] = qf[a][1]; qf[a][1] = t; t = qf[a][2]; qf[a][2] = qf[a][3]; qf[a][3] = t; }
    if (w & 2) { bf16x8 t = qf[a][0]; qf[a][0] = qf[a][2]; qf[a][2] = t; t = qf[a][1]; qf[a][1] = qf[a][3]; qf[a][3] = t; }
  }
  wait_bar<0>();
  issue_kv(1); issue_kv(2); issue_kv(3);
#pragma unroll
  for (int s = 0; s < 4; ++s) qf[2][s] = lds_b128(lb + Q8OFF + s * 1024 + lane * 16);

  f32x4 oa[3][8];
#pragma unroll
  for (int t = 0; t < 8; ++t) { oa[0][t] = f32x4{0, 0, 0, 0}; oa[1][t] = f32x4{0, 0, 0, 0}; oa[2][t] = f32x4{0, 0, 0, 0}; }
  float nm[3] = {0.f, 0.f, 0.f}, ls[3] = {0.f, 0.f, 0.f};
  const unsigned ak = lb + (c16 >> 3) * 2048 + (c16 & 7) * 64 + ((g ^ ((c16 & 8) >> 2)) << 4);
  const unsigned av = lb + (g >> 1) * 2048 + (4 * (g & 1) + (c16 >> 2)) * 64 + (g & 1) * 32 + 8 * (c16 & 3);
#pragma clang loop unroll(disable)
  for (int n = 0; n < 8; ++n) {
    int nn = n; asm volatile("" : "+s"(nn));
    const bool first = nn == 0;
    const int tk = 2 * n, tv = 2 * n + 1;
    if (nn < 7) wait_bar<12>(); else wait_bar<4>();
    if (nn < 6) issue_kv(tk + 4);
    const unsigned ko = (tk % 5) * SLOT, vo = (tv % 5) * SLOT;
    f32x4 sa[2][4], s8;
#pragma unroll
    for (int kt = 0; kt < 4; ++kt) {
      sa[0][kt] = f32x4{nm[0], nm[0], nm[0], nm[0]}; sa[1][kt] = f32x4{nm[1], nm[1], nm[1], nm[1]};
      if (kt == 0) s8 = f32x4{nm[2], nm[2], nm[2], nm[2]};
      const unsigned kb = ak + (ko + (kt ^ w) * 4096);
#pragma unroll
      for (int s = 0; s < 4; ++s) {
        const bf16x8 kf = lds_b128(kb + s * 512);
        sa[0][kt] = mfma(kf, qf[0][s], sa[0][kt]);
        sa[1][kt] = mfma(kf, qf[1][s], sa[1][kt]);
        if (kt == 0) s8 = mfma(kf, qf[2][s], s8);
      }
    }
    float pm0 = fmaxf(fmaxf(max4(sa[0][0]), max4(sa[0][1])), fmaxf(max4(sa[0][2]), max4(sa[0][3])));
    float pm1 = fmaxf(fmaxf(max4(sa[1][0]), max4(sa[1][1])), fmaxf(max4(sa[1][2]), max4(sa[1][3])));
    float pm2 = max4(s8);
    if (__builtin_expect(first || __any(fmaxf(fmaxf(pm0, pm1), pm2) > THR), 0)) {
      float d0 = gmax(pm0), d1 = gmax(pm1), d2 = gmax(pm2);
      const float e0 = fmaxf(d0, 0.f), e1 = fmaxf(d1, 0.f), e2 = fmaxf(d2, 0.f);
      if (!first) { d0 = e0; d1 = e1; d2 = e2; }
      const float a0 = __builtin_amdgcn_exp2f(-e0), a1 = __builtin_amdgcn_exp2f(-e1), a2 = __builtin_amdgcn_exp2f(-e2);
      ls[0] *= a0; ls[1] *= a1; ls[2] *= a2;
#pragma unroll
      for (int t = 0; t < 8; ++t) { oa[0][t] *= a0; oa[1][t] *= a1; oa[2][t] *= a2; }
      nm[0] -= d0; nm[1] -= d1; nm[2] -= d2;
#pragma unroll
      for (int kt = 0; kt < 4; ++kt) { sa[0][kt] -= d0; sa[1][kt] -= d1; }
      s8 -= d2;
    }
    bf16x8 pf[2][2], p8;
#pragma unroll
    for (int a = 0; a < 2; ++a) {
#pragma unroll
      for (int kt = 0; kt < 4; ++kt)
#pragma unroll
        for (int r = 0; r < 4; ++r) { sa[a][kt][r] = __builtin_amdgcn_exp2f(sa[a][kt][r]); ls[a] += sa[a][kt][r]; }
      pf[a][0] = pack8(sa[a][0], sa[a][1]); pf[a][1] = pack8(sa[a][2], sa[a][3]);
    }
#pragma unroll
    for (int r = 0; r < 4; ++r) { s8[r] = __builtin_amdgcn_exp2f(s8[r]); ls[2] += s8[r]; }
    { u32x4 t = {pk2(s8[0], s8[1]), pk2(s8[2], s8[3]), 0u, 0u}; p8 = __builtin_bit_cast(bf16x8, t); }
    if (nn < 6) { wait_bar<12>(); issue_kv(tv + 4); } else if (nn == 6) wait_bar<8>(); else wait_bar<0>();
#pragma unroll
    for (int m = 0; m < 2; ++m) {
      const unsigned v0 = av + (vo + ((2 * m) ^ w) * 4096), v1 = av + (vo + ((2 * m + 1) ^ w) * 4096);
      const unsigned v0x = v0 ^ 32, v1x = v1 ^ 32;
#pragma unroll
      for (int t = 0; t < 8; ++t) {
        const bf16x8 vf = lds_tr2(((t & 1) ? v0x : v0) + (t >> 1) * 512, ((t & 1) ? v1x : v1) + (t >> 1) * 512);
        oa[0][t] = mfma(vf, pf[0][m], oa[0][t]);
        oa[1][t] = mfma(vf, pf[1][m], oa[1][t]);
        if (m == 0) oa[2][t] = mfma(vf, p8, oa[2][t]);
      }
    }
    if (n == 0) { STAMPAT(0, 2); }
  }
  STAMPAT(0, 3);
  u32x4* ob = ows + ((size_t)(br * NB + b) * 9 * NH + h) * 256 + lane;
  const unsigned mb = lb + SLOT;
  const float l8 = gsum(ls[2]);
#pragma unroll
  for (int t = 0; t < 8; ++t) *(LDS_AS f32x4*)(uintptr_t)(mb + w * 8192 + t * 1024 + lane * 16) = oa[2][t];
  *(LDS_AS f32x2*)(uintptr_t)(mb + 32768 + w * 512 + lane * 8) = f32x2{nm[2], l8};
#pragma unroll
  for (int a = 0; a < 2; ++a) {
    const float inv = __builtin_amdgcn_rcpf(gsum(ls[a]));
#pragma unroll
    for (int s = 0; s < 4; ++s) ob[(size_t)(2 * w + a) * NH * 256 + s * 64] = __builtin_bit_cast(u32x4, pack8(oa[a][2 * s] * inv, oa[a][2 * s + 1] * inv));
  }
  asm volatile("s_waitcnt lgkmcnt(0)\n\ts_barrier" ::: "memory");
  {
    f32x2 ml[4];
#pragma unroll
    for (int i = 0; i < 4; ++i) ml[i] = *(const LDS_AS f32x2*)(uintptr_t)(mb + 32768 + i * 512 + lane * 8);
    const float nmin = fminf(fminf(ml[0][0], ml[1][0]), fminf(ml[2][0], ml[3][0]));
    float sc[4], l = 0.f;
#pragma unroll
    for (int i = 0; i < 4; ++i) { sc[i] = __builtin_amdgcn_exp2f(nmin - ml[i][0]); l += sc[i] * ml[i][1]; }
    const float inv = __builtin_amdgcn_rcpf(l);
    f32x4 o0 = {0, 0, 0, 0}, o1 = {0, 0, 0, 0};
#pragma unroll
    for (int i = 0; i < 4; ++i) {
      o0 += *(const LDS_AS f32x4*)(uintptr_t)(mb + i * 8192 + (2 * w) * 1024 + lane * 16) * (sc[i] * inv);
      o1 += *(const LDS_AS f32x4*)(uintptr_t)(mb + i * 8192 + (2 * w + 1) * 1024 + lane * 16) * (sc[i] * inv);
    }
    ob[(size_t)8 * NH * 256 + w * 64] = __builtin_bit_cast(u32x4, pack8(o0, o1));
  }
  STAMPAT(0, 4);
}

constexpr int OST = 36864;
template <int NQ, int NN>
__device__ __forceinline__ void oproj_body(unsigned lb, int w, int lane, int q0, int n0, rsrc_t ro, rsrc_t rw, float* __restrict__ part) {
  const int c16 = lane & 15, g = lane >> 4, kh = w >> 2;
  unsigned vo_w[3];
#pragma unroll
  for (int i = 0; i < 3; ++i) { const int c = (w + 8 * i) * 64 + lane; vo_w[i] = (c / 18) * (DM * 2) + (c % 18) * 16; }
  auto issue = [&](int st, int buf) {
    const unsigned sb = lb + buf * OST;
    dma16<0>(ro, lane * 16, (w >> 1) * NH * 4096 + st * 2048 + (w & 1) * 1024, sb + w * 1024);
    dma16<0>(ro, lane * 16, (4 + (w >> 1)) * NH * 4096 + st * 2048 + (w & 1) * 1024, sb + (8 + w) * 1024);
    dma16<0>(rw, vo_w[0], st * 64 * DM * 2, sb + 18432 + w * 1024);
    dma16<0>(rw, vo_w[1], st * 64 * DM * 2, sb + 18432 + (8 + w) * 1024);
    if (w < 2) dma16<0>(rw, vo_w[2], st * 64 * DM * 2, sb + 18432 + (16 + w) * 1024);
    else if (w < 4) dma16<0>(ro, lane * 16, 8 * NH * 4096 + st * 2048 + (w & 1) * 1024, sb + (16 + (w & 1)) * 1024);
  };
  f32x4 acc[NQ][NN];
#pragma unroll
  for (int a = 0; a < NQ; ++a)
#pragma unroll
    for (int t = 0; t < NN; ++t) acc[a][t] = f32x4{0, 0, 0, 0};
  const unsigned ao = lb + (q0 * 2 + kh) * 1024 + lane * 16;
  const unsigned aw = lb + 18432 + (32 * kh + 4 * g + (c16 >> 2)) * 288 + n0 * 32 + 8 * (c16 & 3);
  auto comp = [&](int buf) {
    bf16x8 of[NQ];
#pragma unroll
    for (int a = 0; a < NQ; ++a) of[a] = lds_b128(ao + buf * OST + a * 2048);
#pragma unroll
    for (int t = 0; t < NN; ++t) {
      const bf16x8 wf = lds_tr2(aw + buf * OST + t * 32, aw + buf * OST + t * 32 + 16 * 288);
#pragma unroll
      for (int a = 0; a < NQ; ++a) acc[a][t] = mfma(wf, of[a], acc[a][t]);
    }
  };
  auto wb = [&](auto nc) { constexpr int n = decltype(nc)::value; if (kh == 0) wait_bar<5 * n>(); else wait_bar<4 * n>(); };
  using std::integral_constant;
  issue(0, 0); issue(1, 1); issue(2, 2);
  for (int it = 0; it < 3; ++it) {
#pragma unroll
    for (int u = 0; u < 4; ++u) { wb(integral_constant<int, 2>{}); issue(4 * it + u + 3, (u + 3) & 3); comp(u); }
  }
  wb(integral_constant<int, 2>{}); issue(15, 3); comp(0);
  wb(integral_constant<int, 2>{}); comp(1);
  wb(integral_constant<int, 1>{}); comp(2);
  wb(integral_constant<int, 0>{}); comp(3);
  if (kh == 1) {
#pragma unroll
    for (int a = 0; a < NQ; ++a)
#pragma unroll
      for (int t = 0; t < NN; ++t) *(LDS_AS f32x4*)(uintptr_t)(lb + ((q0 + a) * 9 + n0 + t) * 1024 + lane * 16) = acc[a][t];
  }
  asm volatile("s_waitcnt lgkmcnt(0)\n\ts_barrier" ::: "memory");
  if (kh == 0) {
#pragma unroll
    for (int a = 0; a < NQ; ++a)
#pragma unroll
      for (int t = 0; t < NN; ++t) {
        const f32x4 v = acc[a][t] + *(const LDS_AS f32x4*)(uintptr_t)(lb + ((q0 + a) * 9 + n0 + t) * 1024 + lane * 16);
        *(f32x4*)(part + ((q0 + a) * 9 + n0 + t) * 256 + lane * 4) = v;
      }
  }
}
__global__ __launch_bounds__(512, 2) void oproj_kernel(const void* woa, const void* wob, const void* __restrict__ ows, float* __restrict__ part STAMP_ARG) {
  __shared__ __attribute__((aligned(16))) char smem[4 * OST];
  const unsigned lb = (unsigned)(uintptr_t)(LDS_AS char*)smem;
  STAMPAT(1, 0);
  const int lane = threadIdx.x & 63, w = __builtin_amdgcn_readfirstlane(threadIdx.x >> 6);
  const int xcd = blockIdx.x & 7, j = blockIdx.x >> 3;
  const int br = xcd >> 2, kq = xcd & 3, b = j & 7, nq = j >> 3;
  const rsrc_t ro = mk_rsrc((const char*)ows + ((size_t)(br * NB + b) * 9 * NH + kq * 8) * 4096, 9 * NH * 4096 - kq * 8 * 4096);
  const rsrc_t rw = mk_rsrc((const char*)(br ? wob : woa) + (size_t)kq * 1024 * DM * 2 + nq * 288, 1024 * DM * 2 - nq * 288);
  float* pp = part + (size_t)(((br * NB + b) * 4 + nq) * 4 + kq) * 81 * 256;
  const int quad = (w & 3) ^ ((w >> 2) * 3);
  if (quad == 0) oproj_body<5, 5>(lb, w, lane, 0, 0, ro, rw, pp);
  else if (quad == 1) oproj_body<5, 4>(lb, w, lane, 0, 5, ro, rw, pp);
  else if (quad == 2) oproj_body<4, 5>(lb, w, lane, 5, 0, ro, rw, pp);
  else oproj_body<4, 4>(lb, w, lane, 5, 5, ro, rw, pp);
  STAMPAT(1, 1);
}

__global__ __launch_bounds__(256) void reduce_kernel(const f32x4* __restrict__ part, u32x2* __restrict__ out STAMP_ARG) {
  STAMPAT(2, 0);
  const int lane = threadIdx.x & 63;
  const int tile = blockIdx.x * 4 + (threadIdx.x >> 6);
  const int grp = tile / 81, tt = tile % 81, qt = tt / 9, nt = tt % 9, bb = grp >> 2, nq = grp & 3, br = bb >> 3, b = bb & 7;
  const f32x4* p = part + ((size_t)grp * 4 * 81 + tt) * 64 + lane;
  const f32x4 v = (p[0] + p[81 * 64]) + (p[2 * 81 * 64] + p[3 * 81 * 64]);
  const int row = b * SQ + qt * 16 + (lane & 15), col = br * DM + nq * 144 + nt * 16 + 4 * (lane >> 4);
  out[(row * 2 * DM + col) >> 2] = u32x2{pk2(v[0], v[1]), pk2(v[2], v[3])};
  STAMPAT(2, 1);
}

extern "C" void kernel_launch(void* const* d_in, const int* in_sizes, int n_in, void* d_out, int out_size, void* d_ws, size_t ws_size, hipStream_t stream) {
  Ptrs pa = {d_in[0], d_in[1], d_in[2], d_in[3], d_in[4]}, pb = {d_in[5], d_in[6], d_in[7], d_in[8], d_in[9]};
  char* ws = (char*)d_ws;
  attn_kernel<<<512, 256, 0, stream>>>(pa, pb, (u32x4*)ws STAMP_PASS);
  oproj_kernel<<<256, 512, 0, stream>>>(d_in[4], d_in[9], ws, (float*)(ws + (32u << 20)) STAMP_PASS);
  reduce_kernel<<<16 * 81, 256, 0, stream>>>((const f32x4*)(ws + (32u << 20)), (u32x2*)d_out STAMP_PASS);
}
```

```cpp
#include <hip/hip_runtime.h>
#include <hip/hip_bf16.h>
#include <stdint.h>

typedef __attribute__((ext_vector_type(8))) __bf16 bf16x8;
typedef __attribute__((ext_vector_type(4))) __bf16 bf16x4;
typedef __attribute__((ext_vector_type(2))) __bf16 bf16x2;
typedef __attribute__((ext_vector_type(4))) float f32x4;
typedef __attribute__((ext_vector_type(2))) float f32x2;
typedef __attribute__((ext_vector_type(4))) unsigned u32x4;
typedef __attribute__((ext_vector_type(2))) unsigned u32x2;
#define LDS_AS __attribute__((address_space(3)))
typedef __amdgpu_buffer_rsrc_t rsrc_t;

constexpr int NB = 8, SQ = 144, DM = 576, NH = 32, SK = 512, DH = 128, HD = NH * DH;

#define STAMPAT(kern, idx)
#define STAMP_ARG
#define STAMP_PASS
#define STAMP_FWD

__device__ __forceinline__ rsrc_t mk_rsrc(const void* p, int bytes) {
  return __builtin_amdgcn_make_buffer_rsrc((void*)p, (short)0, bytes, 0x00020000);
}
template <int NT>
__device__ __forceinline__ void dma16(rsrc_t r, unsigned voff, unsigned soff, unsigned ldsb) {
  if constexpr (NT)
    asm volatile("s_mov_b32 m0, %0\n\ts_nop 0\n\tbuffer_load_dwordx4 %1, %2, %3 offen nt lds" ::"s"(ldsb), "v"(voff), "s"(r), "s"(soff) : "memory");
  else
    asm volatile("s_mov_b32 m0, %0\n\ts_nop 0\n\tbuffer_load_dwordx4 %1, %2, %3 offen lds" ::"s"(ldsb), "v"(voff), "s"(r), "s"(soff) : "memory");
}
template <int NT>
__device__ __forceinline__ void dma4(rsrc_t r, unsigned voff, unsigned soff, unsigned ldsb) {
  if constexpr (NT)
    asm volatile("s_mov_b32 m0, %0\n\ts_nop 0\n\tbuffer_load_dword %1, %2, %3 offen nt lds" ::"s"(ldsb), "v"(voff), "s"(r), "s"(soff) : "memory");
  else
    asm volatile("s_mov_b32 m0, %0\n\ts_nop 0\n\tbuffer_load_dword %1, %2, %3 offen lds" ::"s"(ldsb), "v"(voff), "s"(r), "s"(soff) : "memory");
}
template <int NT>
__device__ __forceinline__ void dma16_if(rsrc_t r, unsigned voff, unsigned soff, unsigned ldsb, int a, int b) {
  if constexpr (NT)
    asm volatile("s_cmp_lg_u32 %4, %5\n\ts_cbranch_scc1 .Lsk%=\n\ts_mov_b32 m0, %0\n\ts_nop 0\n\tbuffer_load_dwordx4 %1, %2, %3 offen nt lds\n.Lsk%=:" ::"s"(ldsb), "v"(voff), "s"(r), "s"(soff), "s"(a), "s"(b) : "memory", "scc");
  else
    asm volatile("s_cmp_lg_u32 %4, %5\n\ts_cbranch_scc1 .Lsk%=\n\ts_mov_b32 m0, %0\n\ts_nop 0\n\tbuffer_load_dwordx4 %1, %2, %3 offen lds\n.Lsk%=:" ::"s"(ldsb), "v"(voff), "s"(r), "s"(soff), "s"(a), "s"(b) : "memory", "scc");
}
__device__ __forceinline__ void dma16nt_lt(rsrc_t r, unsigned voff, unsigned soff, unsigned ldsb, int add, int a, int b) {
  unsigned t;
  asm volatile("s_cmp_ge_u32 %5, %6\n\ts_cbranch_scc1 .Lsk%=\n\ts_add_u32 m0, %1, %7\n\ts_add_u32 %0, %4, %7\n\tbuffer_load_dwordx4 %2, %3, %0 offen nt lds\n.Lsk%=:" : "=&s"(t) : "s"(ldsb), "v"(voff), "s"(r), "s"(soff), "s"(a), "i"(b), "i"(add) : "memory", "scc");
}
__device__ __forceinline__ void dma4_eq(rsrc_t r, unsigned voff, unsigned ldsb, int a, int b) {
  asm volatile("s_cmp_lg_u32 %3, %4\n\ts_cbranch_scc1 .Lsk%=\n\ts_mov_b32 m0, %0\n\ts_nop 0\n\tbuffer_load_dword %1, %2, 0 offen lds\n.Lsk%=:" ::"s"(ldsb), "v"(voff), "s"(r), "s"(a), "i"(b) : "memory", "scc");
}
#define SB() __builtin_amdgcn_sched_barrier(0)
template <int N>
__device__ __forceinline__ void wait_bar() {
  asm volatile("s_waitcnt vmcnt(%0) lgkmcnt(0)\n\ts_barrier" ::"i"(N) : "memory");
}
template <int N0, int N>
__device__ __forceinline__ void wait_bar2(int a, int b) {
  asm volatile("s_cmp_eq_u32 %2, %3\n\ts_cbranch_scc1 .Lw2%=\n\ts_waitcnt vmcnt(%1)\n.Lw2%=:\n\ts_waitcnt vmcnt(%0) lgkmcnt(0)\n\ts_barrier" ::"i"(N0), "i"(N), "s"(a), "s"(b) : "memory", "scc");
}
template <int N0>
__device__ __forceinline__ void wait_bar3(int c, int a, int b) {
  asm volatile("s_cmp_lt_u32 %1, 2\n\ts_cbranch_scc1 .Lw3%=\n\ts_waitcnt vmcnt(4)\n\ts_cmp_eq_u32 %2, %3\n\ts_cbranch_scc1 .Lw3%=\n\ts_waitcnt vmcnt(3)\n.Lw3%=:\n\ts_waitcnt vmcnt(%0) lgkmcnt(0)\n\ts_barrier" ::"i"(N0), "s"(c), "s"(a), "s"(b) : "memory", "scc");
}
__device__ __forceinline__ bf16x8 lds_b128(unsigned a) { return *(const LDS_AS bf16x8*)(uintptr_t)a; }
__device__ __forceinline__ bf16x4 lds_tr(unsigned a) { return __builtin_amdgcn_ds_read_tr16_b64_v4bf16((LDS_AS bf16x4*)(uintptr_t)a); }
__device__ __forceinline__ bf16x8 lds_tr2(unsigned a0, unsigned a1) {
  bf16x4 x = lds_tr(a0), y = lds_tr(a1);
  return __builtin_shufflevector(x, y, 0, 1, 2, 3, 4, 5, 6, 7);
}
__device__ __forceinline__ f32x4 mfma(bf16x8 a, bf16x8 b, f32x4 c) { return __builtin_amdgcn_mfma_f32_16x16x32_bf16(a, b, c, 0, 0, 0); }
__device__ __forceinline__ void mfma_p(bf16x8 a, bf16x8 b, f32x4& c) { c = mfma(a, b, c); asm volatile("" : "+v"(c)); }
__device__ __forceinline__ unsigned pk2(float a, float b) {
  f32x2 v = {a, b};
  bf16x2 r = __builtin_convertvector(v, bf16x2);
  return __builtin_bit_cast(unsigned, r);
}
__device__ __forceinline__ bf16x8 pack8(f32x4 a, f32x4 b) {
  u32x4 w = {pk2(a[0], a[1]), pk2(a[2], a[3]), pk2(b[0], b[1]), pk2(b[2], b[3])};
  return __builtin_bit_cast(bf16x8, w);
}
__device__ __forceinline__ float fmx(float a, float b) { return __builtin_elementwise_maximum(a, b); }
__device__ __forceinline__ float gmax(float v) {
  auto a = __builtin_amdgcn_permlane16_swap(__float_as_uint(v), __float_as_uint(v), false, false);
  v = fmx(__uint_as_float(a[0]), __uint_as_float(a[1]));
  auto b = __builtin_amdgcn_permlane32_swap(__float_as_uint(v), __float_as_uint(v), false, false);
  return fmx(__uint_as_float(b[0]), __uint_as_float(b[1]));
}
__device__ __forceinline__ float gsum(float v) {
  auto a = __builtin_amdgcn_permlane16_swap(__float_as_uint(v), __float_as_uint(v), false, false);
  v = __uint_as_float(a[0]) + __uint_as_float(a[1]);
  auto b = __builtin_amdgcn_permlane32_swap(__float_as_uint(v), __float_as_uint(v), false, false);
  return __uint_as_float(b[0]) + __uint_as_float(b[1]);
}
__device__ __forceinline__ float max4(f32x4 a) { return fmx(fmx(a[0], a[1]), fmx(a[2], a[3])); }

#define NT_KV 1
#define PP 1
#define GSL SLOT
#define BURST 1
#define LIM 6
#define EXPF __builtin_amdgcn_exp2f
#define KS 0
#define VS0 6
#define K0L 0
#define WG0 6
#define NT_W 0
constexpr float QSCALE = 0.08838834764831845f * 1.4426950408889634f;
constexpr float PLIM = 4096.f;
constexpr int SLOT = 16384, HALF = 81920, XB0 = 49152, XB1 = HALF + XB0, Q8OFF = 77824;
__device__ __forceinline__ constexpr int wbuf(int i) { return i == 0 ? 32768 : i == 1 ? 40960 : 67584; }

struct Ptrs { const void *x, *k, *v, *wq, *wo; };

__global__ __launch_bounds__(512) void attn_kernel(Ptrs pa, Ptrs pb, u32x4* __restrict__ ows, unsigned* __restrict__ flags STAMP_ARG) {
  __shared__ __attribute__((aligned(16))) char smem[2 * HALF];
  const int lane = threadIdx.x & 63, w8 = __builtin_amdgcn_readfirstlane(threadIdx.x >> 6), w = w8 & 3, hh = w8 >> 2;
  const unsigned lx = (unsigned)(uintptr_t)(LDS_AS char*)smem, lb = lx + hh * HALF;
  STAMPAT(0, 0);
  const int c16 = lane & 15, g = lane >> 4;
  const int xcd = blockIdx.x & 7, j = blockIdx.x >> 3;
  const int br = xcd >> 2, b = j & 7, hl = j >> 3, h = (xcd & 3) * 8 + 2 * hl + hh;
  const Ptrs p = br ? pb : pa;
  const rsrc_t rx = mk_rsrc((const char*)p.x + (size_t)b * SQ * DM * 2, SQ * DM * 2);
  const rsrc_t rw = mk_rsrc((const char*)p.wq + h * DH * 2, DM * HD * 2 - h * DH * 2);
  const rsrc_t rk = mk_rsrc((const char*)p.k + (size_t)(b * NH + h) * SK * DH * 2, SK * DH * 2);
  const rsrc_t rv = mk_rsrc((const char*)p.v + (size_t)(b * NH + h) * SK * DH * 2, SK * DH * 2);

  const unsigned fw = (g & 1) | ((g & 2) << 1) | ((w & 1) << 3);
  const unsigned vo_w = (8 * w + g) * (HD * 2) + ((c16 ^ fw) << 4);
  const unsigned vo_x0 = (lane >> 3) * (DM * 2) + (((lane & 7) ^ g) << 4), vo_x1 = (vo_x0 ^ 64) + 8 * DM * 2;
  const int r8 = (lane >> 2) & 7;
  const unsigned vo_kv = (8 * (w >> 1) + r8) * 256 + (2 * (w & 1) + (lane >> 5)) * 64;
  const unsigned vo_k = vo_kv + (((lane & 3) ^ (w & 2)) << 4);
  const unsigned vo_v = vo_kv + (((lane & 3) ^ ((r8 & 4) >> 1)) << 4);

  auto iss_w = [&](int kc, int buf, int i) { dma16<NT_W>(rw, vo_w, (kc * 32 + 4 * i) * HD * 2, lb + wbuf(buf) + w * 2048 + i * 1024); };
  auto iss_x = [&](int kp, unsigned xb, int i) { dma16<NT_W>(rx, i ? vo_x1 : vo_x0, kp * 128 + w8 * 16 * DM * 2, xb + w8 * 2048 + i * 1024); };
  auto iss_x8 = [&](int kp, unsigned xb, int i, int who) { dma16_if<NT_W>(rx, i ? vo_x1 : vo_x0, kp * 128 + 128 * DM * 2, xb + 16384 + i * 1024, w8, who); };
  auto iss_kv = [&](int t, int i) {
    const unsigned sb = lb + (t % 5) * SLOT + w * 1024, so = (t >> 1) * SLOT;
    if (t & 1) dma16<NT_KV>(rv, vo_v, so + i * 4096, sb + i * 4096);
    else dma16<NT_KV>(rk, vo_k, so + i * 4096, sb + i * 4096);
  };

  int kp = b + 2 * hl; kp = kp >= 9 ? kp - 9 : kp;
  int kc = 2 * kp;
  auto nkc = [&]() { kc = kc == 17 ? 0 : kc + 1; };
  auto nkp = [&]() { kp = kp == 8 ? 0 : kp + 1; };
  iss_x8(kp, lx + XB0, 0, 0); iss_x8(kp, lx + XB0, 1, 1);
  iss_x(kp, lx + XB0, 0); iss_x(kp, lx + XB0, 1); nkp();
  iss_w(kc, 0, 0); iss_w(kc, 0, 1); nkc();
  iss_x8(kp, lx + XB1, 0, 2); iss_x8(kp, lx + XB1, 1, 3);
  iss_w(kc, 1, 0); iss_w(kc, 1, 1); nkc(); iss_x(kp, lx + XB1, 0); iss_x(kp, lx + XB1, 1); nkp();
  iss_w(kc, 2, 0); iss_w(kc, 2, 1); nkc();
  f32x4 qa[2][8], q8[2];
#pragma unroll
  for (int t = 0; t < 8; ++t) { qa[0][t] = f32x4{0, 0, 0, 0}; qa[1][t] = f32x4{0, 0, 0, 0}; }
  q8[0] = f32x4{0, 0, 0, 0}; q8[1] = f32x4{0, 0, 0, 0};
  unsigned aw[8];
  {
    const int qq = c16 >> 2, pp = c16 & 3;
    const unsigned f = (qq & 1) | ((qq & 2) << 1) | ((g & 1) << 3);
#pragma unroll
    for (int s = 0; s < 4; ++s) { aw[2 * s] = lb + wbuf(0) + (8 * g + qq) * 256 + ((((unsigned)(4 * (s ^ w) + pp)) ^ f) << 4) + 8 * (pp & 1); aw[2 * s + 1] = aw[2 * s] ^ 8; }
  }
  const unsigned axl = c16 * 128 + ((g ^ (c16 >> 1)) << 4);
  unsigned xrd = lx + XB0, xwr = lx + XB0;
  bf16x8 fx[2][3], fy[3]; bf16x4 fq[2][8][2];
  unsigned ax[4];
  auto x_addr = [&]() { ax[0] = axl + (xrd + w * 4096); ax[1] = ax[0] ^ 64; ax[2] = axl + (xrd + 16384); ax[3] = ax[2] ^ 64; xrd = 2 * lx + XB0 + XB1 - xrd; };
  auto rd_w = [&](int set, int buf, int i) { fq[set][i >> 1][i & 1] = lds_tr(aw[i >> 1] + (wbuf(buf) - wbuf(0)) + (i & 1) * 1024); };
  auto rd_x = [&](int i) {
    const bf16x8 v = lds_b128(i % 3 == 2 ? ax[2 + i / 3] : ax[i / 3] + (i % 3) * 2048);
    if (i < 3) fx[0][i] = v; else fy[i - 3] = v;
  };
  x_addr();
  wait_bar<6>();
  STAMPAT(0, 5);
#pragma unroll
  for (int i = 0; i < 16; ++i) rd_w(0, 0, i);
#pragma unroll
  for (int i = 0; i < 6; ++i) rd_x(i);
  if (!K0L) { SB(); iss_kv(0, 0); iss_kv(0, 1); iss_kv(0, 2); iss_kv(0, 3); SB(); }
  auto q_step = [&](auto uc, auto tailc, int who, int prev) {
    constexpr int u = decltype(uc)::value, cs = u & 1; constexpr bool TAIL = decltype(tailc)::value;
    if (cs) {
#pragma unroll
      for (int i = 0; i < 3; ++i) fx[1][i] = fy[i];
      x_addr();
    }
    if (!K0L && !TAIL && u < 2) wait_bar3<u ? 7 : 8>(who, w8, prev);
    else if (!TAIL || u < 3) wait_bar2<4, 3>(w8, prev);
    else if (u < 5) wait_bar<u == 3 ? 6 : 4 + 4 * K0L>();
    int gap = 0;
    auto fill = [&]() {
      SB();
      if (!(TAIL && u == 5)) {
        constexpr int G0 = WG0;
        if (gap >= G0 && gap < G0 + 8) { rd_w(cs ^ 1, (u + 1) % 3, 2 * (gap - G0)); rd_w(cs ^ 1, (u + 1) % 3, 2 * (gap - G0) + 1); }
        else if (cs && gap < 14) rd_x(gap < G0 ? gap : gap - 8);
      }
      if (!TAIL || u < 2) {
        {if (gap == 2) iss_x8(kp, xwr, cs, who);
        if (gap == 5 || gap == 9) iss_w(kc, u % 3, gap == 9);
        if (gap == 13) iss_x(kp, xwr, cs);}
      } else if (u == 2) {
        if (gap == 2 || gap == 5) iss_w(kc, u % 3, gap == 5);
        if (gap >= 8 && gap % 3 == 2) iss_kv(1 - K0L, (gap - 8) / 3);
      } else if (u > 3 - K0L && gap >= 2 && gap < 14 && gap % 3 == 2) iss_kv(u - 2, (gap - 2) / 3);
      ++gap;
      SB();
    };
#pragma unroll
    for (int f = 0; f < 8; ++f) {
      const bf16x8 wf = __builtin_shufflevector(fq[cs][f][0], fq[cs][f][1], 0, 1, 2, 3, 4, 5, 6, 7);
      mfma_p(wf, fx[cs][0], qa[0][f]); fill();
      mfma_p(wf, fx[cs][1], qa[1][f]); fill();
      if (f < 2) { mfma_p(wf, fx[cs][2], q8[f]); fill(); }
    }
    nkc();
    if (cs) { nkp(); xwr = 2 * lx + XB0 + XB1 - xwr; }
  };
  using std::integral_constant;
#define QS(u, tl, who, prev) q_step(integral_constant<int, u>{}, integral_constant<bool, tl>{}, who, prev)
#pragma clang loop unroll(disable)
  for (int it = 0; it < 8; it += 4) { QS(0, false, it, 7); QS(1, false, it + 1, it); QS(2, false, it + 2, it + 1); QS(3, false, it + 3, it + 2); QS(4, false, it ^ 6, it + 3); QS(5, false, it ^ 7, it ^ 6); }
  STAMPAT(0, 6);
  QS(0, true, 4, 3); QS(1, true, 5, 4); QS(2, true, 0, 5); QS(3, true, 0, 0); STAMPAT(0, 7); QS(4, true, 0, 0); QS(5, true, 0, 0);
  STAMPAT(0, 1);
  bf16x8 qf[3][4];
  auto qpack = [&](f32x4 lo, f32x4 hi) {
    lo *= QSCALE; hi *= QSCALE;
    const unsigned a0 = pk2(lo[0], lo[1]), a1 = pk2(lo[2], lo[3]), b0 = pk2(hi[0], hi[1]), b1 = pk2(hi[2], hi[3]);
    const bool odd = g & 1;
    u32x4 r = {odd ? b0 : a0, odd ? b1 : a1, odd ? a0 : b0, odd ? a1 : b1};
    return __builtin_bit_cast(bf16x8, r);
  };
  *(LDS_AS bf16x8*)(uintptr_t)(lb + Q8OFF + w * 1024 + lane * 16) = qpack(q8[0], q8[1]);
#pragma unroll
  for (int s = 0; s < 4; ++s) { qf[0][s] = qpack(qa[0][2 * s], qa[0][2 * s + 1]); qf[1][s] = qpack(qa[1][2 * s], qa[1][2 * s + 1]); }
#pragma unroll
  for (int a = 0; a < 2; ++a) {
    if (w & 1) { bf16x8 t = qf[a][0]; qf[a][0] = qf[a][1]; qf[a][1] = t; t = qf[a][2]; qf[a][2] = qf[a][3]; qf[a][3] = t; }
    if (w & 2) { bf16x8 t = qf[a][0]; qf[a][0] = qf[a][2]; qf[a][2] = t; t = qf[a][1]; qf[a][1] = qf[a][3]; qf[a][3] = t; }
  }
  wait_bar<12>();
#pragma unroll
  for (int s = 0; s < 4; ++s) qf[2][s] = lds_b128(lb + Q8OFF + s * 1024 + lane * 16);
  if (PP && hh) asm volatile("s_barrier" ::: "memory");
  f32x4 oa[3][8];
#pragma unroll
  for (int t = 0; t < 8; ++t) { oa[0][t] = f32x4{0, 0, 0, 0}; oa[1][t] = f32x4{0, 0, 0, 0}; oa[2][t] = f32x4{0, 0, 0, 0}; }
  float nm[3] = {0.f, 0.f, 0.f}, ls[3] = {0.f, 0.f, 0.f};
  const unsigned ak = lb + (c16 >> 3) * 2048 + (c16 & 7) * 64 + ((g ^ ((c16 & 8) >> 2)) << 4);
  const unsigned av = lb + (g >> 1) * 2048 + (4 * (g & 1) + (c16 >> 2)) * 64 + (g & 1) * 32 + 8 * (c16 & 3);
  const rsrc_t rp = mk_rsrc((const char*)p.wo + ((size_t)h * DH + b * 16) * DM * 2 + w * 4608, 4608);
  unsigned ko = 0;
#pragma clang loop unroll(disable)
  for (int n0 = 0; n0 < 8; ++n0) {
    int n = n0; asm volatile("" : "+s"(n));
    const unsigned vo = ko == 4 * SLOT ? 0 : ko + SLOT, po = ko == 0 ? 4 * SLOT : ko - SLOT;
    unsigned kb[4];
#pragma unroll
    for (int kt = 0; kt < 4; ++kt) kb[kt] = ak + (ko + (kt ^ w) * 4096);
    asm volatile("s_cmp_lt_u32 %0, 7\n\ts_cbranch_scc1 .Lwa%=\n\ts_waitcnt vmcnt(5)\n.Lwa%=:\n\ts_waitcnt vmcnt(12) lgkmcnt(0)\n\ts_barrier" ::"s"(n) : "memory", "scc");
    bf16x8 kf[2][4];
#pragma unroll
    for (int s = 0; s < 4; ++s) kf[0][s] = lds_b128(kb[0] + s * 512);
    SB();
    unsigned gso = (n + 2) * GSL, kld = lb + po + w * 1024, vld = lb + ko + w * 1024;
    asm volatile("" : "+s"(gso), "+s"(kld), "+s"(vld));
    auto iss_k = [&](int i, int lim) { dma16nt_lt(rk, vo_k, gso, kld, i * 4096, n, lim); };
    f32x4 sa[2][4], s8;
    float ts[3];
    bf16x8 pf[2][2], p8;
    auto ex = [&](f32x4& v, float& t, bool init) {
#pragma unroll
      for (int r = 0; r < 4; ++r) { v[r] = EXPF(v[r]); if (init && r == 0) t = v[0]; else t += v[r]; }
    };
    auto packs = [&]() {
      pf[0][0] = pack8(sa[0][0], sa[0][1]); pf[1][0] = pack8(sa[1][0], sa[1][1]); pf[0][1] = pack8(sa[0][2], sa[0][3]); pf[1][1] = pack8(sa[1][2], sa[1][3]);
      u32x4 t = {pk2(s8[0], s8[1]), pk2(s8[2], s8[3]), 0u, 0u}; p8 = __builtin_bit_cast(bf16x8, t);
    };
    bool ok = false;
    if (n != 0) {
#pragma unroll
      for (int kt = 0; kt < 4; ++kt) {
        sa[0][kt] = f32x4{nm[0], nm[0], nm[0], nm[0]}; sa[1][kt] = f32x4{nm[1], nm[1], nm[1], nm[1]};
        if (kt == 0) s8 = f32x4{nm[2], nm[2], nm[2], nm[2]};
#pragma unroll
        for (int s = 0; s < 4; ++s) {
          SB();
          sa[0][kt] = mfma(kf[kt & 1][s], qf[0][s], sa[0][kt]);
          if (kt < 3) {
 kf[(kt & 1) ^ 1][s] = lds_b128(kb[kt + 1] + s * 512);
}
          sa[1][kt] = mfma(kf[kt & 1][s], qf[1][s], sa[1][kt]);
          if (kt == 0) s8 = mfma(kf[0][s], qf[2][s], s8);
          if (BURST ? kt == KS : s == KS) { SB(); iss_k(BURST ? s : kt, LIM); if ((BURST ? s : kt) == 0) dma4_eq(rp, lane * 128, lb + SLOT + w * 256, n, 6); }
          if (kt > 0 && s == 1) ex(sa[0][kt - 1], ts[0], kt == 1);
          if (kt > 0 && s == 2) ex(sa[1][kt - 1], ts[1], kt == 1);
          if (kt == 1 && s == 3) ex(s8, ts[2], true);
        }
      }
      SB();
      ex(sa[0][3], ts[0], false); ex(sa[1][3], ts[1], false);
      packs();
      ok = !__any(!(ts[0] + ts[1] + ts[2] <= PLIM));
    }
    if (__builtin_expect(!ok, 0)) {
      asm volatile("" : "+v"(kb[0]), "+v"(kb[1]), "+v"(kb[2]), "+v"(kb[3]));
#pragma unroll
      for (int kt = 0; kt < 4; ++kt) {
        sa[0][kt] = f32x4{nm[0], nm[0], nm[0], nm[0]}; sa[1][kt] = f32x4{nm[1], nm[1], nm[1], nm[1]};
        if (kt == 0) s8 = f32x4{nm[2], nm[2], nm[2], nm[2]};
#pragma unroll
        for (int s = 0; s < 4; ++s) kf[0][s] = lds_b128(kb[kt] + s * 512);
        iss_k(kt, 1);
#pragma unroll
        for (int s = 0; s < 4; ++s) {
          sa[0][kt] = mfma(kf[0][s], qf[0][s], sa[0][kt]);
          sa[1][kt] = mfma(kf[0][s], qf[1][s], sa[1][kt]);
          if (kt == 0) s8 = mfma(kf[0][s], qf[2][s], s8);
        }
      }
      float d0 = gmax(fmx(fmx(max4(sa[0][0]), max4(sa[0][1])), fmx(max4(sa[0][2]), max4(sa[0][3]))));
      float d1 = gmax(fmx(fmx(max4(sa[1][0]), max4(sa[1][1])), fmx(max4(sa[1][2]), max4(sa[1][3]))));
      float d2 = gmax(max4(s8));
      if (n != 0) {
        d0 = fmx(d0, 0.f); d1 = fmx(d1, 0.f); d2 = fmx(d2, 0.f);
        const float a0 = __builtin_amdgcn_exp2f(-d0), a1 = __builtin_amdgcn_exp2f(-d1), a2 = __builtin_amdgcn_exp2f(-d2);
        ls[0] *= a0; ls[1] *= a1; ls[2] *= a2;
#pragma unroll
        for (int t = 0; t < 8; ++t) { oa[0][t] *= a0; oa[1][t] *= a1; oa[2][t] *= a2; }
      }
      nm[0] -= d0; nm[1] -= d1; nm[2] -= d2;
#pragma unroll
      for (int kt = 0; kt < 4; ++kt) { sa[0][kt] -= d0; sa[1][kt] -= d1; ex(sa[0][kt], ts[0], kt == 0); ex(sa[1][kt], ts[1], kt == 0); }
      s8 -= d2; ex(s8, ts[2], true);
      packs();
    }
    ls[0] += ts[0]; ls[1] += ts[1]; ls[2] += ts[2];
    unsigned vb[4];
#pragma unroll
    for (int i = 0; i < 4; ++i) vb[i] = av + (vo + (i ^ w) * 4096);
    asm volatile("s_cmp_lt_u32 %0, 6\n\ts_cbranch_scc1 .Lwb%=\n\ts_waitcnt vmcnt(9)\n\ts_cmp_lt_u32 %0, 7\n\ts_cbranch_scc1 .Lwb%=\n\ts_waitcnt vmcnt(1)\n.Lwb%=:\n\ts_waitcnt vmcnt(12) lgkmcnt(0)\n\ts_barrier" ::"s"(n) : "memory", "scc");
    bf16x8 vf[4];
    auto rd_v = [&](int i) { const int m = i >> 3, t = (i & 3) * 2 + ((i >> 2) & 1); vf[i & 3] = lds_tr2((vb[2 * m] ^ ((t & 1) * 32)) + (t >> 1) * 512, (vb[2 * m + 1] ^ ((t & 1) * 32)) + (t >> 1) * 512); };
    rd_v(0); rd_v(1); rd_v(2);
    SB();
#pragma unroll
    for (int i = 0; i < 16; ++i) {
      const int m = i >> 3, t = (i & 3) * 2 + ((i >> 2) & 1);
      SB();
      oa[0][t] = mfma(vf[i & 3], pf[0][m], oa[0][t]);
      if (i < 13) rd_v(i + 3);
      oa[1][t] = mfma(vf[i & 3], pf[1][m], oa[1][t]);
      if (m == 0) oa[2][t] = mfma(vf[i & 3], p8, oa[2][t]);
      if (BURST ? (i >= VS0 && i < VS0 + 4) : (i == VS0 || i == VS0 + 3 || i == VS0 + 6 || i == VS0 + 10)) { SB(); const int q = (i - VS0) / (BURST ? 1 : 3); dma16nt_lt(rv, vo_v, gso, vld, q * 4096, n, LIM); }
    }
    SB();
    ko = vo == 4 * SLOT ? 0 : vo + SLOT;
    if (n == 0) { STAMPAT(0, 2); }
  }
  STAMPAT(0, 3);
  u32x4* ob = ows + ((size_t)(br * NB + b) * 9 * NH + h) * 256 + lane;
  if (threadIdx.x == 0) flags[blockIdx.x * 32] = 0;
  const unsigned mb = lb + SLOT + 1024;
  const float l8 = gsum(ls[2]);
#pragma unroll
  for (int t = 0; t < 8; ++t) *(LDS_AS f32x4*)(uintptr_t)(mb + w * 8192 + t * 1024 + lane * 16) = oa[2][t];
  *(LDS_AS f32x2*)(uintptr_t)(mb + 32768 + w * 512 + lane * 8) = f32x2{nm[2], l8};
#pragma unroll
  for (int a = 0; a < 2; ++a) {
    const float inv = __builtin_amdgcn_rcpf(gsum(ls[a]));
#pragma unroll
    for (int s = 0; s < 4; ++s) ob[(size_t)(2 * w + a) * NH * 256 + s * 64] = __builtin_bit_cast(u32x4, pack8(oa[a][2 * s] * inv, oa[a][2 * s + 1] * inv));
  }
  asm volatile("s_waitcnt lgkmcnt(0)\n\ts_barrier" ::: "memory");
  {
    f32x2 ml[4];
#pragma unroll
    for (int i = 0; i < 4; ++i) ml[i] = *(const LDS_AS f32x2*)(uintptr_t)(mb + 32768 + i * 512 + lane * 8);
    const float nmin = fminf(fminf(ml[0][0], ml[1][0]), fminf(ml[2][0], ml[3][0]));
    float sc[4], l = 0.f;
#pragma unroll
    for (int i = 0; i < 4; ++i) { sc[i] = __builtin_amdgcn_exp2f(nmin - ml[i][0]); l += sc[i] * ml[i][1]; }
    const float inv = __builtin_amdgcn_rcpf(l);
    f32x4 o0 = {0, 0, 0, 0}, o1 = {0, 0, 0, 0};
#pragma unroll
    for (int i = 0; i < 4; ++i) {
      o0 += *(const LDS_AS f32x4*)(uintptr_t)(mb + i * 8192 + (2 * w) * 1024 + lane * 16) * (sc[i] * inv);
      o1 += *(const LDS_AS f32x4*)(uintptr_t)(mb + i * 8192 + (2 * w + 1) * 1024 + lane * 16) * (sc[i] * inv);
    }
    ob[(size_t)8 * NH * 256 + w * 64] = __builtin_bit_cast(u32x4, pack8(o0, o1));
  }
  if (PP && !hh) asm volatile("s_barrier" ::: "memory");
  STAMPAT(0, 4);
}

constexpr int OST = 36864;
template <int W, int Q0, int NQ, int N0, int NN, bool CEN>
__device__ __forceinline__ void oproj_body(unsigned lb, int lane, int kq, rsrc_t rx, u32x2* __restrict__ out STAMP_ARG) {
  const int c16 = lane & 15, g = lane >> 4;
  constexpr int NW = NN + CEN, NM = NQ * NN + CEN;
  f32x4 acc[NM];
#pragma unroll
  for (int m = 0; m < NM; ++m) acc[m] = f32x4{0, 0, 0, 0};
  const unsigned ao0 = lb + Q0 * 2048 + lane * 16;
  const unsigned aw0 = lb + 18432 + (4 * g + (c16 >> 2)) * 288 + N0 * 32 + 8 * (c16 & 3);
  unsigned ao = ao0, aw = aw0;
  bf16x8 of[2][NQ]; bf16x4 wf[2][NW][2];
  auto rd = [&](int ks, int i) {
    const int f = i / 3, r = i % 3;
    if (r == 2) { if (f < NQ) of[ks][f] = lds_b128(ao + f * 2048 + ks * 1024); }
    else if (f < NW) wf[ks][f][r] = lds_tr(aw + f * 32 + (ks * 32 + 16 * r) * 288);
  };
  auto half = [&](int ks) {
#pragma unroll
    for (int m = 0; m < NM; ++m) {
      SB();
      const int t = m < NQ * NN ? m / NQ : NW - 1, a = m < NQ * NN ? m % NQ : NQ - 1;
      acc[m] = mfma(__builtin_shufflevector(wf[ks][t][0], wf[ks][t][1], 0, 1, 2, 3, 4, 5, 6, 7), of[ks][a], acc[m]);
      SB();
      if (m < 15) rd(ks ^ 1, m);
    }
    SB();
  };
  asm volatile("s_barrier" ::: "memory");
  STAMPAT(1, 3);
#pragma unroll
  for (int i = 0; i < 15; ++i) rd(0, i);
#pragma clang loop unroll(disable)
  for (int t = 1; t <= 16; ++t) {
    half(0);
    ao = ao0 + (t & 3) * OST; aw = aw0 + (t & 3) * OST;
    asm volatile("s_waitcnt lgkmcnt(0)\n\ts_barrier" : "+v"(ao), "+v"(aw) :: "memory");
    half(1);
  }
  STAMPAT(1, 1);
  constexpr int NP = (NM + 1) / 2;
  auto fin = [&](auto kc) {
    constexpr int K = decltype(kc)::value;
#pragma unroll
    for (int p = 0; p < NP; ++p)
      if (((p + 2 * W) & 3) != K) __builtin_amdgcn_raw_buffer_store_b128(__builtin_bit_cast(u32x4, pack8(acc[2 * p], acc[2 * p + 1 < NM ? 2 * p + 1 : 2 * p])), rx, lane * 16, ((K * 4 + W) * 11 + p) * 1024, 16);
    asm volatile("s_waitcnt vmcnt(0)\n\ts_barrier" ::: "memory");
    STAMPAT(1, 4);
    asm volatile("s_barrier" ::: "memory");
    STAMPAT(1, 5);
    u32x4 in[3][3];
#pragma unroll
    for (int p = 0; p < NP; ++p)
      if (((p + 2 * W) & 3) == K) {
#pragma unroll
        for (int i = 0; i < 3; ++i) in[p >> 2][i] = __builtin_amdgcn_raw_buffer_load_b128(rx, lane * 16, ((((K + 1 + i) & 3) * 4 + W) * 11 + p) * 1024, 16);
      }
#pragma unroll
    for (int m = 0; m < NM; ++m)
      if ((((m >> 1) + 2 * W) & 3) == K) {
        f32x4 v = acc[m];
#pragma unroll
        for (int i = 0; i < 3; ++i) {
          const unsigned u0 = in[m >> 3][i][2 * (m & 1)], u1 = in[m >> 3][i][2 * (m & 1) + 1];
          v += f32x4{__uint_as_float(u0 << 16), __uint_as_float(u0 & 0xffff0000u), __uint_as_float(u1 << 16), __uint_as_float(u1 & 0xffff0000u)};
        }
        const int qt = m < NQ * NN ? Q0 + m % NQ : 4, nt = m < NQ * NN ? N0 + m / NQ : 4;
        out[(qt * 16 + c16) * (2 * DM / 4) + nt * 4 + g] = u32x2{pk2(v[0], v[1]), pk2(v[2], v[3])};
      }
  };
  using std::integral_constant;
  if (kq == 0) fin(integral_constant<int, 0>{}); else if (kq == 1) fin(integral_constant<int, 1>{});
  else if (kq == 2) fin(integral_constant<int, 2>{}); else fin(integral_constant<int, 3>{});
}
__global__ __launch_bounds__(512) void oproj_kernel(const void* woa, const void* wob, const void* __restrict__ ows, char* xch, unsigned* flags, u32x2* __restrict__ out STAMP_ARG) {
  __shared__ __attribute__((aligned(16))) char smem[4 * OST];
  const unsigned lb = (unsigned)(uintptr_t)(LDS_AS char*)smem;
  STAMPAT(1, 0);
  const int lane = threadIdx.x & 63, w = __builtin_amdgcn_readfirstlane(threadIdx.x >> 6);
  const int xcd = blockIdx.x & 7, j = blockIdx.x >> 3;
  const int br = xcd >> 2, kq = xcd & 3, b = j & 7, nq = j >> 3;
  if (w >= 4) {
    const bool iw = w >= 6;
    const char* src = iw ? (const char*)(br ? wob : woa) + (size_t)kq * 1024 * DM * 2 + nq * 288 : (const char*)ows + ((size_t)(br * NB + b) * 9 * NH + kq * 8) * 4096;
    const rsrc_t r = mk_rsrc(src, iw ? 1024 * DM * 2 - nq * 288 : 9 * NH * 4096 - kq * 8 * 4096);
    const unsigned sst = iw ? 64 * DM * 2 : 2048;
    unsigned vo[9];
#pragma unroll
    for (int i = 0; i < 9; ++i) {
      const int p = (w & 1) * 9 + i, c = p * 64 + lane;

      vo[i] = iw ? (c / 18) * (DM * 2) + (c % 18) * 16 :
 (p >> 1) * NH * 4096 + (p & 1) * 1024 + lane * 16;
    }
#define ROTX (2 * b)
    int st = ROTX;
    auto issue = [&](int buf) {
      const unsigned sb = lb + buf * OST + (w - 4) * 9216, so = st * sst;
#pragma unroll
      for (int i = 0; i < 9; ++i) {
        dma16<0>(r, vo[i], so, sb + i * 1024);
      }
      st = (st + 1) & 15;
    };
    issue(0); issue(1); wait_bar<9>(); issue(2); issue(3);
#pragma clang loop unroll(disable)
    for (int t = 0; t < 12; ++t) { wait_bar<18>(); issue(t & 3); }
    wait_bar<18>(); wait_bar<9>(); wait_bar<0>(); wait_bar<0>();
    asm volatile("s_barrier" ::: "memory");
    if (w == 4) {
      unsigned* f = flags + (blockIdx.x & ~3u) * 32;
      if (lane == 0) __hip_atomic_store(f + kq * 32, 1u, __ATOMIC_RELAXED, __HIP_MEMORY_SCOPE_AGENT);
      while (__any(__hip_atomic_load(f + (lane & 3) * 32, __ATOMIC_RELAXED, __HIP_MEMORY_SCOPE_AGENT) != 1u)) __builtin_amdgcn_s_sleep(2);
    }
    asm volatile("s_barrier" ::: "memory");
    return;
  }
  const rsrc_t rx = mk_rsrc(xch + (size_t)(blockIdx.x >> 2) * 176 * 1024, 176 * 1024);
  u32x2* po = out + (b * SQ * 2 * DM + br * DM + nq * 144) / 4;
  if (w == 0) oproj_body<0, 0, 5, 0, 4, true>(lb, lane, kq, rx, po STAMP_FWD);
  else if (w == 1) oproj_body<1, 0, 4, 4, 5, false>(lb, lane, kq, rx, po STAMP_FWD);
  else if (w == 2) oproj_body<2, 4, 5, 5, 4, false>(lb, lane, kq, rx, po STAMP_FWD);
  else oproj_body<3, 5, 4, 0, 5, false>(lb, lane, kq, rx, po STAMP_FWD);
  STAMPAT(1, 2);
}

extern "C" void kernel_launch(void* const* d_in, const int* in_sizes, int n_in, void* d_out, int out_size, void* d_ws, size_t ws_size, hipStream_t stream) {
  Ptrs pa = {d_in[0], d_in[1], d_in[2], d_in[3], d_in[4]}, pb = {d_in[5], d_in[6], d_in[7], d_in[8], d_in[9]};
  char* ws = (char*)d_ws;
  unsigned* flags = (unsigned*)(ws + (64u << 20));
  attn_kernel<<<256, 512, 0, stream>>>(pa, pb, (u32x4*)ws, flags STAMP_PASS);
  oproj_kernel<<<256, 512, 0, stream>>>(d_in[4], d_in[9], ws, ws + (32u << 20), flags, (u32x2*)d_out STAMP_PASS);
}
```

```cpp
#include <hip/hip_runtime.h>
#include <hip/hip_bf16.h>
#include <stdint.h>

typedef __attribute__((ext_vector_type(8))) __bf16 bf16x8;
typedef __attribute__((ext_vector_type(4))) __bf16 bf16x4;
typedef __attribute__((ext_vector_type(2))) __bf16 bf16x2;
typedef __attribute__((ext_vector_type(4))) float f32x4;
typedef __attribute__((ext_vector_type(2))) float f32x2;
typedef __attribute__((ext_vector_type(4))) unsigned u32x4;
typedef __attribute__((ext_vector_type(2))) unsigned u32x2;
#define LDS_AS __attribute__((address_space(3)))
typedef __amdgpu_buffer_rsrc_t rsrc_t;

constexpr int NB = 8, SQ = 144, DM = 576, NH = 32, SK = 512, DH = 128, HD = NH * DH;

#define STAMPAT(kern, idx)
#define STAMP_ARG
#define STAMP_PASS
#define STAMP_FWD

__device__ __forceinline__ rsrc_t mk_rsrc(const void* p, int bytes) {
  return __builtin_amdgcn_make_buffer_rsrc((void*)p, (short)0, bytes, 0x00020000);
}
template <int NT>
__device__ __forceinline__ void dma16(rsrc_t r, unsigned voff, unsigned soff, unsigned ldsb) {
  if constexpr (NT == 2)
    asm volatile("s_mov_b32 m0, %0\n\ts_nop 0\n\tbuffer_load_dwordx4 %1, %2, %3 offen sc1 lds" ::"s"(ldsb), "v"(voff), "s"(r), "s"(soff) : "memory");
  else if constexpr (NT)
    asm volatile("s_mov_b32 m0, %0\n\ts_nop 0\n\tbuffer_load_dwordx4 %1, %2, %3 offen nt lds" ::"s"(ldsb), "v"(voff), "s"(r), "s"(soff) : "memory");
  else
    asm volatile("s_mov_b32 m0, %0\n\ts_nop 0\n\tbuffer_load_dwordx4 %1, %2, %3 offen lds" ::"s"(ldsb), "v"(voff), "s"(r), "s"(soff) : "memory");
}
template <int NT>
__device__ __forceinline__ void dma4(rsrc_t r, unsigned voff, unsigned soff, unsigned ldsb) {
  if constexpr (NT)
    asm volatile("s_mov_b32 m0, %0\n\ts_nop 0\n\tbuffer_load_dword %1, %2, %3 offen nt lds" ::"s"(ldsb), "v"(voff), "s"(r), "s"(soff) : "memory");
  else
    asm volatile("s_mov_b32 m0, %0\n\ts_nop 0\n\tbuffer_load_dword %1, %2, %3 offen lds" ::"s"(ldsb), "v"(voff), "s"(r), "s"(soff) : "memory");
}
template <int NT>
__device__ __forceinline__ void dma16_if(rsrc_t r, unsigned voff, unsigned soff, unsigned ldsb, int a, int b) {
  if constexpr (NT)
    asm volatile("s_cmp_lg_u32 %4, %5\n\ts_cbranch_scc1 .Lsk%=\n\ts_mov_b32 m0, %0\n\ts_nop 0\n\tbuffer_load_dwordx4 %1, %2, %3 offen nt lds\n.Lsk%=:" ::"s"(ldsb), "v"(voff), "s"(r), "s"(soff), "s"(a), "s"(b) : "memory", "scc");
  else
    asm volatile("s_cmp_lg_u32 %4, %5\n\ts_cbranch_scc1 .Lsk%=\n\ts_mov_b32 m0, %0\n\ts_nop 0\n\tbuffer_load_dwordx4 %1, %2, %3 offen lds\n.Lsk%=:" ::"s"(ldsb), "v"(voff), "s"(r), "s"(soff), "s"(a), "s"(b) : "memory", "scc");
}
__device__ __forceinline__ void dma16nt_lt(rsrc_t r, unsigned voff, unsigned soff, unsigned ldsb, int add, int a, int b) {
  unsigned t;
  asm volatile("s_cmp_ge_u32 %5, %6\n\ts_cbranch_scc1 .Lsk%=\n\ts_add_u32 m0, %1, %7\n\ts_add_u32 %0, %4, %7\n\tbuffer_load_dwordx4 %2, %3, %0 offen nt lds\n.Lsk%=:" : "=&s"(t) : "s"(ldsb), "v"(voff), "s"(r), "s"(soff), "s"(a), "i"(b), "i"(add) : "memory", "scc");
}
__device__ __forceinline__ void dma4_eq(rsrc_t r, unsigned voff, unsigned ldsb, int a, int b) {
  asm volatile("s_cmp_lg_u32 %3, %4\n\ts_cbranch_scc1 .Lsk%=\n\ts_mov_b32 m0, %0\n\ts_nop 0\n\tbuffer_load_dword %1, %2, 0 offen lds\n.Lsk%=:" ::"s"(ldsb), "v"(voff), "s"(r), "s"(a), "i"(b) : "memory", "scc");
}
#define SB() __builtin_amdgcn_sched_barrier(0)
template <int N>
__device__ __forceinline__ void wait_bar() {
  asm volatile("s_waitcnt vmcnt(%0) lgkmcnt(0)\n\ts_barrier" ::"i"(N) : "memory");
}
template <int N0, int N>
__device__ __forceinline__ void wait_bar2(int a, int b) {
  asm volatile("s_cmp_eq_u32 %2, %3\n\ts_cbranch_scc1 .Lw2%=\n\ts_waitcnt vmcnt(%1)\n.Lw2%=:\n\ts_waitcnt vmcnt(%0) lgkmcnt(0)\n\ts_barrier" ::"i"(N0), "i"(N), "s"(a), "s"(b) : "memory", "scc");
}
template <int N0>
__device__ __forceinline__ void wait_bar3(int c, int a, int b) {
  asm volatile("s_cmp_lt_u32 %1, 2\n\ts_cbranch_scc1 .Lw3%=\n\ts_waitcnt vmcnt(4)\n\ts_cmp_eq_u32 %2, %3\n\ts_cbranch_scc1 .Lw3%=\n\ts_waitcnt vmcnt(3)\n.Lw3%=:\n\ts_waitcnt vmcnt(%0) lgkmcnt(0)\n\ts_barrier" ::"i"(N0), "s"(c), "s"(a), "s"(b) : "memory", "scc");
}
__device__ __forceinline__ bf16x8 lds_b128(unsigned a) { return *(const LDS_AS bf16x8*)(uintptr_t)a; }
__device__ __forceinline__ bf16x4 lds_tr(unsigned a) { return __builtin_amdgcn_ds_read_tr16_b64_v4bf16((LDS_AS bf16x4*)(uintptr_t)a); }
__device__ __forceinline__ bf16x8 lds_tr2(unsigned a0, unsigned a1) {
  bf16x4 x = lds_tr(a0), y = lds_tr(a1);
  return __builtin_shufflevector(x, y, 0, 1, 2, 3, 4, 5, 6, 7);
}
__device__ __forceinline__ f32x4 mfma(bf16x8 a, bf16x8 b, f32x4 c) { return __builtin_amdgcn_mfma_f32_16x16x32_bf16(a, b, c, 0, 0, 0); }
__device__ __forceinline__ void mfma_p(bf16x8 a, bf16x8 b, f32x4& c) { c = mfma(a, b, c); asm volatile("" : "+v"(c)); }
__device__ __forceinline__ unsigned pk2(float a, float b) {
  f32x2 v = {a, b};
  bf16x2 r = __builtin_convertvector(v, bf16x2);
  return __builtin_bit_cast(unsigned, r);
}
__device__ __forceinline__ bf16x8 pack8(f32x4 a, f32x4 b) {
  u32x4 w = {pk2(a[0], a[1]), pk2(a[2], a[3]), pk2(b[0], b[1]), pk2(b[2], b[3])};
  return __builtin_bit_cast(bf16x8, w);
}
__device__ __forceinline__ float fmx(float a, float b) { return __builtin_elementwise_maximum(a, b); }
__device__ __forceinline__ float gmax(float v) {
  auto a = __builtin_amdgcn_permlane16_swap(__float_as_uint(v), __float_as_uint(v), false, false);
  v = fmx(__uint_as_float(a[0]), __uint_as_float(a[1]));
  auto b = __builtin_amdgcn_permlane32_swap(__float_as_uint(v), __float_as_uint(v), false, false);
  return fmx(__uint_as_float(b[0]), __uint_as_float(b[1]));
}
__device__ __forceinline__ float gsum(float v) {
  auto a = __builtin_amdgcn_permlane16_swap(__float_as_uint(v), __float_as_uint(v), false, false);
  v = __uint_as_float(a[0]) + __uint_as_float(a[1]);
  auto b = __builtin_amdgcn_permlane32_swap(__float_as_uint(v), __float_as_uint(v), false, false);
  return __uint_as_float(b[0]) + __uint_as_float(b[1]);
}
__device__ __forceinline__ float max4(f32x4 a) { return fmx(fmx(a[0], a[1]), fmx(a[2], a[3])); }

#define NT_KV 1
#define PP 1
#define GSL SLOT
#define BURST 1
#define LIM 6
#define EXPF __builtin_amdgcn_exp2f
#define CODE 51200
#define KS 0
#define VS0 6
#define K0L 0
#define WG0 6
#define NT_W 0
constexpr float QSCALE = 0.08838834764831845f * 1.4426950408889634f;
constexpr float PLIM = 4096.f;
constexpr int SLOT = 16384, HALF = 81920, XB0 = 49152, XB1 = HALF + XB0, Q8OFF = 77824;
__device__ __forceinline__ constexpr int wbuf(int i) { return i == 0 ? 32768 : i == 1 ? 40960 : 67584; }

struct Ptrs { const void *x, *k, *v, *wq, *wo; };

constexpr int WX = 18432, OY = 3 * WX;
constexpr unsigned TAG = 0x9E3779B9u;
template <int W, int Q0, int NQ, int N0, int NN, bool CEN>
__device__ __forceinline__ void oproj_body(unsigned lb, int lane, int kq, rsrc_t rx, u32x2* __restrict__ out STAMP_ARG) {
  const int c16 = lane & 15, g = lane >> 4;
  constexpr int NW = NN + CEN, NM = NQ * NN + CEN;
  f32x4 acc[NM];
#pragma unroll
  for (int m = 0; m < NM; ++m) acc[m] = f32x4{0, 0, 0, 0};
  const unsigned ao0 = lb + Q0 * 2048 + lane * 16;
  const unsigned aw0 = lb + 18432 + (4 * g + (c16 >> 2)) * 288 + N0 * 32 + 8 * (c16 & 3);
  unsigned ao = ao0, aw = aw0;
  bf16x8 of[2][NQ]; bf16x4 wf[2][NW][2];
  auto rd = [&](int ks, int i) {
    const int f = i / 3, r = i % 3;
    if (r == 2) { if (f < NQ) of[ks][f] = lds_b128(ao + f * 2048 + ks * 1024); }
    else if (f < NW) wf[ks][f][r] = lds_tr(aw + f * 32 + (ks * 32 + 16 * r) * 288);
  };
  auto half = [&](int ks) {
#pragma unroll
    for (int m = 0; m < NM; ++m) {
      SB();
      const int t = m < NQ * NN ? m / NQ : NW - 1, a = m < NQ * NN ? m % NQ : NQ - 1;
      acc[m] = mfma(__builtin_shufflevector(wf[ks][t][0], wf[ks][t][1], 0, 1, 2, 3, 4, 5, 6, 7), of[ks][a], acc[m]);
      SB();
      if (m < 15) rd(ks ^ 1, m);
    }
    SB();
  };
  STAMPAT(1, 3);
#pragma unroll
  for (int i = 0; i < 15; ++i) rd(0, i);
#pragma clang loop unroll(disable)
  for (int t = 1; t <= 16; ++t) {
    half(0);
    ao = ao0 + ((t & 1) * HALF + (t & 2) * (OY / 2)); aw = aw0 + ((t & 1) * HALF + (t & 2) * (WX / 2));
    asm volatile("s_cmp_lg_u32 %2, 2\n\ts_cbranch_scc1 .Lo%=\n\ts_waitcnt vmcnt(0)\n.Lo%=:\n\ts_waitcnt lgkmcnt(0)\n\ts_barrier" : "+v"(ao), "+v"(aw) : "s"(t) : "memory", "scc");
    half(1);
  }
  STAMPAT(1, 1);
  constexpr int NP = (NM + 1) / 2;
  auto fin = [&](auto kc) {
    constexpr int K = decltype(kc)::value;
#pragma unroll
    for (int p = 0; p < NP; ++p)
      if (((p + 2 * W) & 3) != K) __builtin_amdgcn_raw_buffer_store_b128(__builtin_bit_cast(u32x4, pack8(acc[2 * p], acc[2 * p + 1 < NM ? 2 * p + 1 : 2 * p])), rx, lane * 16, ((K * 4 + W) * 11 + p) * 1024, 16);
    asm volatile("s_waitcnt vmcnt(0)\n\ts_barrier" ::: "memory");
    STAMPAT(1, 4);
    asm volatile("s_barrier" ::: "memory");
    STAMPAT(1, 5);
    u32x4 in[3][3];
#pragma unroll
    for (int p = 0; p < NP; ++p)
      if (((p + 2 * W) & 3) == K) {
#pragma unroll
        for (int i = 0; i < 3; ++i) in[p >> 2][i] = __builtin_amdgcn_raw_buffer_load_b128(rx, lane * 16, ((((K + 1 + i) & 3) * 4 + W) * 11 + p) * 1024, 16);
      }
#pragma unroll
    for (int m = 0; m < NM; ++m)
      if ((((m >> 1) + 2 * W) & 3) == K) {
        f32x4 v = acc[m];
#pragma unroll
        for (int i = 0; i < 3; ++i) {
          const unsigned u0 = in[m >> 3][i][2 * (m & 1)], u1 = in[m >> 3][i][2 * (m & 1) + 1];
          v += f32x4{__uint_as_float(u0 << 16), __uint_as_float(u0 & 0xffff0000u), __uint_as_float(u1 << 16), __uint_as_float(u1 & 0xffff0000u)};
        }
        const int qt = m < NQ * NN ? Q0 + m % NQ : 4, nt = m < NQ * NN ? N0 + m / NQ : 4;
        out[(qt * 16 + c16) * (2 * DM / 4) + nt * 4 + g] = u32x2{pk2(v[0], v[1]), pk2(v[2], v[3])};
      }
  };
  using std::integral_constant;
  if (kq == 0) fin(integral_constant<int, 0>{}); else if (kq == 1) fin(integral_constant<int, 1>{});
  else if (kq == 2) fin(integral_constant<int, 2>{}); else fin(integral_constant<int, 3>{});
}
__global__ __launch_bounds__(512) void fused_kernel(Ptrs pa, Ptrs pb, char* ows, char* xch, unsigned* flags, u32x2* __restrict__ out STAMP_ARG) {
  __shared__ __attribute__((aligned(16))) char smem[2 * HALF];
  const int lane = threadIdx.x & 63, w8 = __builtin_amdgcn_readfirstlane(threadIdx.x >> 6), w = w8 & 3, hh = w8 >> 2;
  const unsigned lx = (unsigned)(uintptr_t)(LDS_AS char*)smem, lb = lx + hh * HALF;
  STAMPAT(0, 0);
  const int c16 = lane & 15, g = lane >> 4;
  const int xcd = blockIdx.x & 7, j = blockIdx.x >> 3;
  const int br = xcd >> 2, b = j & 7, hl = j >> 3, h = (xcd & 3) * 8 + 2 * hl + hh;
  const Ptrs p = br ? pb : pa;
  const rsrc_t rx = mk_rsrc((const char*)p.x + (size_t)b * SQ * DM * 2, SQ * DM * 2);
  const rsrc_t rw = mk_rsrc((const char*)p.wq + h * DH * 2, DM * HD * 2 - h * DH * 2);
  const rsrc_t rk = mk_rsrc((const char*)p.k + (size_t)(b * NH + h) * SK * DH * 2, SK * DH * 2);
  const rsrc_t rv = mk_rsrc((const char*)p.v + (size_t)(b * NH + h) * SK * DH * 2, SK * DH * 2);

  const unsigned fw = (g & 1) | ((g & 2) << 1) | ((w & 1) << 3);
  const unsigned vo_w = (8 * w + g) * (HD * 2) + ((c16 ^ fw) << 4);
  const unsigned vo_x0 = (lane >> 3) * (DM * 2) + (((lane & 7) ^ g) << 4), vo_x1 = (vo_x0 ^ 64) + 8 * DM * 2;
  const int r8 = (lane >> 2) & 7;
  const unsigned vo_kv = (8 * (w >> 1) + r8) * 256 + (2 * (w & 1) + (lane >> 5)) * 64;
  const unsigned vo_k = vo_kv + (((lane & 3) ^ (w & 2)) << 4);
  const unsigned vo_v = vo_kv + (((lane & 3) ^ ((r8 & 4) >> 1)) << 4);

  auto iss_w = [&](int kc, int buf, int i) { dma16<NT_W>(rw, vo_w, (kc * 32 + 4 * i) * HD * 2, lb + wbuf(buf) + w * 2048 + i * 1024); };
  auto iss_x = [&](int kp, unsigned xb, int i) { dma16<NT_W>(rx, i ? vo_x1 : vo_x0, kp * 128 + w8 * 16 * DM * 2, xb + w8 * 2048 + i * 1024); };
  auto iss_x8 = [&](int kp, unsigned xb, int i, int who) { dma16_if<NT_W>(rx, i ? vo_x1 : vo_x0, kp * 128 + 128 * DM * 2, xb + 16384 + i * 1024, w8, who); };
  auto iss_kv = [&](int t, int i) {
    const unsigned sb = lb + (t % 5) * SLOT + w * 1024, so = (t >> 1) * SLOT;
    if (t & 1) dma16<NT_KV>(rv, vo_v, so + i * 4096, sb + i * 4096);
    else dma16<NT_KV>(rk, vo_k, so + i * 4096, sb + i * 4096);
  };

  {
    unsigned long long pc; asm volatile("s_getpc_b64 %0" : "=s"(pc));
    dma4_eq(mk_rsrc((const char*)(pc & ~127ull), CODE), (j & 7) * 8192 + lane * 128, lb + 75776, w8, 7);
  }
  int kp = b + 2 * hl; kp = kp >= 9 ? kp - 9 : kp;
  int kc = 2 * kp;
  auto nkc = [&]() { kc = kc == 17 ? 0 : kc + 1; };
  auto nkp = [&]() { kp = kp == 8 ? 0 : kp + 1; };
  iss_x8(kp, lx + XB0, 0, 0); iss_x8(kp, lx + XB0, 1, 1);
  iss_x(kp, lx + XB0, 0); iss_x(kp, lx + XB0, 1); nkp();
  iss_w(kc, 0, 0); iss_w(kc, 0, 1); nkc();
  iss_x8(kp, lx + XB1, 0, 2); iss_x8(kp, lx + XB1, 1, 3);
  iss_w(kc, 1, 0); iss_w(kc, 1, 1); nkc(); iss_x(kp, lx + XB1, 0); iss_x(kp, lx + XB1, 1); nkp();
  iss_w(kc, 2, 0); iss_w(kc, 2, 1); nkc();
  f32x4 qa[2][8], q8[2];
#pragma unroll
  for (int t = 0; t < 8; ++t) { qa[0][t] = f32x4{0, 0, 0, 0}; qa[1][t] = f32x4{0, 0, 0, 0}; }
  q8[0] = f32x4{0, 0, 0, 0}; q8[1] = f32x4{0, 0, 0, 0};
  unsigned aw[8];
  {
    const int qq = c16 >> 2, pp = c16 & 3;
    const unsigned f = (qq & 1) | ((qq & 2) << 1) | ((g & 1) << 3);
#pragma unroll
    for (int s = 0; s < 4; ++s) { aw[2 * s] = lb + wbuf(0) + (8 * g + qq) * 256 + ((((unsigned)(4 * (s ^ w) + pp)) ^ f) << 4) + 8 * (pp & 1); aw[2 * s + 1] = aw[2 * s] ^ 8; }
  }
  const unsigned axl = c16 * 128 + ((g ^ (c16 >> 1)) << 4);
  unsigned xrd = lx + XB0, xwr = lx + XB0;
  bf16x8 fx[2][3], fy[3]; bf16x4 fq[2][8][2];
  unsigned ax[4];
  auto x_addr = [&]() { ax[0] = axl + (xrd + w * 4096); ax[1] = ax[0] ^ 64; ax[2] = axl + (xrd + 16384); ax[3] = ax[2] ^ 64; xrd = 2 * lx + XB0 + XB1 - xrd; };
  auto rd_w = [&](int set, int buf, int i) { fq[set][i >> 1][i & 1] = lds_tr(aw[i >> 1] + (wbuf(buf) - wbuf(0)) + (i & 1) * 1024); };
  auto rd_x = [&](int i) {
    const bf16x8 v = lds_b128(i % 3 == 2 ? ax[2 + i / 3] : ax[i / 3] + (i % 3) * 2048);
    if (i < 3) fx[0][i] = v; else fy[i - 3] = v;
  };
  x_addr();
  wait_bar<6>();
  STAMPAT(0, 5);
#pragma unroll
  for (int i = 0; i < 16; ++i) rd_w(0, 0, i);
#pragma unroll
  for (int i = 0; i < 6; ++i) rd_x(i);
  if (!K0L) { SB(); iss_kv(0, 0); iss_kv(0, 1); iss_kv(0, 2); iss_kv(0, 3); SB(); }
  auto q_step = [&](auto uc, auto tailc, int who, int prev) {
    constexpr int u = decltype(uc)::value, cs = u & 1; constexpr bool TAIL = decltype(tailc)::value;
    if (cs) {
#pragma unroll
      for (int i = 0; i < 3; ++i) fx[1][i] = fy[i];
      x_addr();
    }
    if (!K0L && !TAIL && u < 2) wait_bar3<u ? 7 : 8>(who, w8, prev);
    else if (!TAIL || u < 3) wait_bar2<4, 3>(w8, prev);
    else if (u < 5) wait_bar<u == 3 ? 6 : 4 + 4 * K0L>();
    int gap = 0;
    auto fill = [&]() {
      SB();
      if (!(TAIL && u == 5)) {
        constexpr int G0 = WG0;
        if (gap >= G0 && gap < G0 + 8) { rd_w(cs ^ 1, (u + 1) % 3, 2 * (gap - G0)); rd_w(cs ^ 1, (u + 1) % 3, 2 * (gap - G0) + 1); }
        else if (cs && gap < 14) rd_x(gap < G0 ? gap : gap - 8);
      }
      if (!TAIL || u < 2) {
        {if (gap == 2) iss_x8(kp, xwr, cs, who);
        if (gap == 5 || gap == 9) iss_w(kc, u % 3, gap == 9);
        if (gap == 13) iss_x(kp, xwr, cs);}
      } else if (u == 2) {
        if (gap == 2 || gap == 5) iss_w(kc, u % 3, gap == 5);
        if (gap >= 8 && gap % 3 == 2) iss_kv(1 - K0L, (gap - 8) / 3);
      } else if (u > 3 - K0L && gap >= 2 && gap < 14 && gap % 3 == 2) iss_kv(u - 2, (gap - 2) / 3);
      ++gap;
      SB();
    };
#pragma unroll
    for (int f = 0; f < 8; ++f) {
      const bf16x8 wf = __builtin_shufflevector(fq[cs][f][0], fq[cs][f][1], 0, 1, 2, 3, 4, 5, 6, 7);
      mfma_p(wf, fx[cs][0], qa[0][f]); fill();
      mfma_p(wf, fx[cs][1], qa[1][f]); fill();
      if (f < 2) { mfma_p(wf, fx[cs][2], q8[f]); fill(); }
    }
    nkc();
    if (cs) { nkp(); xwr = 2 * lx + XB0 + XB1 - xwr; }
  };
  using std::integral_constant;
#define QS(u, tl, who, prev) q_step(integral_constant<int, u>{}, integral_constant<bool, tl>{}, who, prev)
#pragma clang loop unroll(disable)
  for (int it = 0; it < 8; it += 4) { QS(0, false, it, 7); QS(1, false, it + 1, it); QS(2, false, it + 2, it + 1); QS(3, false, it + 3, it + 2); QS(4, false, it ^ 6, it + 3); QS(5, false, it ^ 7, it ^ 6); }
  STAMPAT(0, 6);
  QS(0, true, 4, 3); QS(1, true, 5, 4); QS(2, true, 0, 5); QS(3, true, 0, 0); STAMPAT(0, 7); QS(4, true, 0, 0); QS(5, true, 0, 0);
  STAMPAT(0, 1);
  bf16x8 qf[3][4];
  auto qpack = [&](f32x4 lo, f32x4 hi) {
    lo *= QSCALE; hi *= QSCALE;
    const unsigned a0 = pk2(lo[0], lo[1]), a1 = pk2(lo[2], lo[3]), b0 = pk2(hi[0], hi[1]), b1 = pk2(hi[2], hi[3]);
    const bool odd = g & 1;
    u32x4 r = {odd ? b0 : a0, odd ? b1 : a1, odd ? a0 : b0, odd ? a1 : b1};
    return __builtin_bit_cast(bf16x8, r);
  };
  *(LDS_AS bf16x8*)(uintptr_t)(lb + Q8OFF + w * 1024 + lane * 16) = qpack(q8[0], q8[1]);
#pragma unroll
  for (int s = 0; s < 4; ++s) { qf[0][s] = qpack(qa[0][2 * s], qa[0][2 * s + 1]); qf[1][s] = qpack(qa[1][2 * s], qa[1][2 * s + 1]); }
#pragma unroll
  for (int a = 0; a < 2; ++a) {
    if (w & 1) { bf16x8 t = qf[a][0]; qf[a][0] = qf[a][1]; qf[a][1] = t; t = qf[a][2]; qf[a][2] = qf[a][3]; qf[a][3] = t; }
    if (w & 2) { bf16x8 t = qf[a][0]; qf[a][0] = qf[a][2]; qf[a][2] = t; t = qf[a][1]; qf[a][1] = qf[a][3]; qf[a][3] = t; }
  }
  wait_bar<12>();
#pragma unroll
  for (int s = 0; s < 4; ++s) qf[2][s] = lds_b128(lb + Q8OFF + s * 1024 + lane * 16);
  f32x4 oa[3][8];
#pragma unroll
  for (int t = 0; t < 8; ++t) { oa[0][t] = f32x4{0, 0, 0, 0}; oa[1][t] = f32x4{0, 0, 0, 0}; oa[2][t] = f32x4{0, 0, 0, 0}; }
  float nm[3] = {0.f, 0.f, 0.f}, ls[3] = {0.f, 0.f, 0.f};
  const unsigned ak = lb + (c16 >> 3) * 2048 + (c16 & 7) * 64 + ((g ^ ((c16 & 8) >> 2)) << 4);
  const unsigned av = lb + (g >> 1) * 2048 + (4 * (g & 1) + (c16 >> 2)) * 64 + (g & 1) * 32 + 8 * (c16 & 3);
  const rsrc_t rp = mk_rsrc((const char*)p.wo + ((size_t)h * DH + b * 16) * DM * 2 + w * 4608, 4608);
  const int kq = xcd & 3;
  const rsrc_t ro = mk_rsrc((const char*)p.wo + (size_t)kq * 1024 * DM * 2 + hl * 288, 1024 * DM * 2 - hl * 288);
  const unsigned vo_a = ((w8 * 64 + lane) / 18) * (DM * 2) + ((w8 * 64 + lane) % 18) * 16, vo_b = ((512 + lane) / 18) * (DM * 2) + ((512 + lane) % 18) * 16;
  unsigned ko = 0;
#pragma clang loop unroll(disable)
  for (int n0 = 0; n0 < 8; ++n0) {
    int n = n0; asm volatile("" : "+s"(n));
    const unsigned vo = ko == 4 * SLOT ? 0 : ko + SLOT, po = ko == 0 ? 4 * SLOT : ko - SLOT;
    unsigned kb[4];
#pragma unroll
    for (int kt = 0; kt < 4; ++kt) kb[kt] = ak + (ko + (kt ^ w) * 4096);
    asm volatile("s_cmp_lt_u32 %0, 7\n\ts_cbranch_scc1 .Lwa%=\n\ts_waitcnt vmcnt(5)\n.Lwa%=:\n\ts_waitcnt vmcnt(12) lgkmcnt(0)\n\ts_barrier" ::"s"(n) : "memory", "scc");
    bf16x8 kf[2][4];
#pragma unroll
    for (int s = 0; s < 4; ++s) kf[0][s] = lds_b128(kb[0] + s * 512);
    SB();
    unsigned gso = (n + 2) * GSL, kld = lb + po + w * 1024, vld = lb + ko + w * 1024;
    asm volatile("" : "+s"(gso), "+s"(kld), "+s"(vld));
    auto iss_k = [&](int i, int lim) { dma16nt_lt(rk, vo_k, gso, kld, i * 4096, n, lim); };
    f32x4 sa[2][4], s8;
    float ts[3];
    bf16x8 pf[2][2], p8;
    auto ex = [&](f32x4& v, float& t, bool init) {
#pragma unroll
      for (int r = 0; r < 4; ++r) { v[r] = EXPF(v[r]); if (init && r == 0) t = v[0]; else t += v[r]; }
    };
    auto packs = [&]() {
      pf[0][0] = pack8(sa[0][0], sa[0][1]); pf[1][0] = pack8(sa[1][0], sa[1][1]); pf[0][1] = pack8(sa[0][2], sa[0][3]); pf[1][1] = pack8(sa[1][2], sa[1][3]);
      u32x4 t = {pk2(s8[0], s8[1]), pk2(s8[2], s8[3]), 0u, 0u}; p8 = __builtin_bit_cast(bf16x8, t);
    };
    bool ok = false;
    if (n != 0) {
#pragma unroll
      for (int kt = 0; kt < 4; ++kt) {
        sa[0][kt] = f32x4{nm[0], nm[0], nm[0], nm[0]}; sa[1][kt] = f32x4{nm[1], nm[1], nm[1], nm[1]};
        if (kt == 0) s8 = f32x4{nm[2], nm[2], nm[2], nm[2]};
#pragma unroll
        for (int s = 0; s < 4; ++s) {
          SB();
          sa[0][kt] = mfma(kf[kt & 1][s], qf[0][s], sa[0][kt]);
          if (kt < 3) {
 kf[(kt & 1) ^ 1][s] = lds_b128(kb[kt + 1] + s * 512);
}
          sa[1][kt] = mfma(kf[kt & 1][s], qf[1][s], sa[1][kt]);
          if (kt == 0) s8 = mfma(kf[0][s], qf[2][s], s8);
          if (BURST ? kt == KS : s == KS) { SB(); iss_k(BURST ? s : kt, LIM); if ((BURST ? s : kt) == 0) dma4_eq(rp, lane * 128, lb + SLOT + w * 256, n, 6); }
          if (kt > 0 && s == 1) ex(sa[0][kt - 1], ts[0], kt == 1);
          if (kt > 0 && s == 2) ex(sa[1][kt - 1], ts[1], kt == 1);
          if (kt == 1 && s == 3) ex(s8, ts[2], true);
        }
      }
      SB();
      ex(sa[0][3], ts[0], false); ex(sa[1][3], ts[1], false);
      packs();
      ok = !__any(!(ts[0] + ts[1] + ts[2] <= PLIM));
    }
    if (__builtin_expect(!ok, 0)) {
      asm volatile("" : "+v"(kb[0]), "+v"(kb[1]), "+v"(kb[2]), "+v"(kb[3]));
#pragma unroll
      for (int kt = 0; kt < 4; ++kt) {
        sa[0][kt] = f32x4{nm[0], nm[0], nm[0], nm[0]}; sa[1][kt] = f32x4{nm[1], nm[1], nm[1], nm[1]};
        if (kt == 0) s8 = f32x4{nm[2], nm[2], nm[2], nm[2]};
#pragma unroll
        for (int s = 0; s < 4; ++s) kf[0][s] = lds_b128(kb[kt] + s * 512);
        iss_k(kt, 1);
#pragma unroll
        for (int s = 0; s < 4; ++s) {
          sa[0][kt] = mfma(kf[0][s], qf[0][s], sa[0][kt]);
          sa[1][kt] = mfma(kf[0][s], qf[1][s], sa[1][kt]);
          if (kt == 0) s8 = mfma(kf[0][s], qf[2][s], s8);
        }
      }
      float d0 = gmax(fmx(fmx(max4(sa[0][0]), max4(sa[0][1])), fmx(max4(sa[0][2]), max4(sa[0][3]))));
      float d1 = gmax(fmx(fmx(max4(sa[1][0]), max4(sa[1][1])), fmx(max4(sa[1][2]), max4(sa[1][3]))));
      float d2 = gmax(max4(s8));
      if (n != 0) {
        d0 = fmx(d0, 0.f); d1 = fmx(d1, 0.f); d2 = fmx(d2, 0.f);
        const float a0 = __builtin_amdgcn_exp2f(-d0), a1 = __builtin_amdgcn_exp2f(-d1), a2 = __builtin_amdgcn_exp2f(-d2);
        ls[0] *= a0; ls[1] *= a1; ls[2] *= a2;
#pragma unroll
        for (int t = 0; t < 8; ++t) { oa[0][t] *= a0; oa[1][t] *= a1; oa[2][t] *= a2; }
      }
      nm[0] -= d0; nm[1] -= d1; nm[2] -= d2;
#pragma unroll
      for (int kt = 0; kt < 4; ++kt) { sa[0][kt] -= d0; sa[1][kt] -= d1; ex(sa[0][kt], ts[0], kt == 0); ex(sa[1][kt], ts[1], kt == 0); }
      s8 -= d2; ex(s8, ts[2], true);
      packs();
    }
    ls[0] += ts[0]; ls[1] += ts[1]; ls[2] += ts[2];
    unsigned vb[4];
#pragma unroll
    for (int i = 0; i < 4; ++i) vb[i] = av + (vo + (i ^ w) * 4096);
    asm volatile("s_cmp_lt_u32 %0, 6\n\ts_cbranch_scc1 .Lwb%=\n\ts_waitcnt vmcnt(9)\n\ts_cmp_lt_u32 %0, 7\n\ts_cbranch_scc1 .Lwb%=\n\ts_waitcnt vmcnt(1)\n.Lwb%=:\n\ts_waitcnt vmcnt(12) lgkmcnt(0)\n\ts_barrier" ::"s"(n) : "memory", "scc");
    bf16x8 vf[4];
    auto rd_v = [&](int i) { const int m = i >> 3, t = (i & 3) * 2 + ((i >> 2) & 1); vf[i & 3] = lds_tr2((vb[2 * m] ^ ((t & 1) * 32)) + (t >> 1) * 512, (vb[2 * m + 1] ^ ((t & 1) * 32)) + (t >> 1) * 512); };
    rd_v(0); rd_v(1); rd_v(2);
    SB();
#pragma unroll
    for (int i = 0; i < 16; ++i) {
      const int m = i >> 3, t = (i & 3) * 2 + ((i >> 2) & 1);
      SB();
      oa[0][t] = mfma(vf[i & 3], pf[0][m], oa[0][t]);
      if (i < 13) rd_v(i + 3);
      oa[1][t] = mfma(vf[i & 3], pf[1][m], oa[1][t]);
      if (m == 0) oa[2][t] = mfma(vf[i & 3], p8, oa[2][t]);
      if (BURST ? (i >= VS0 && i < VS0 + 4) : (i == VS0 || i == VS0 + 3 || i == VS0 + 6 || i == VS0 + 10)) { SB(); const int q = (i - VS0) / (BURST ? 1 : 3); dma16nt_lt(rv, vo_v, gso, vld, q * 4096, n, LIM);
        dma16_if<0>(ro, vo_a, (hl * 256 + (q >> 1) * 128 + (q & 1) * 32) * DM * 2, lx + (q >> 1) * HALF + WX + ((q & 1) * 9 + w8) * 1024, n, 7);
        if (q == 3) dma16_if<0>(ro, vo_b, (hl * 256 + (w >> 1) * 128 + (w & 1) * 32) * DM * 2, lx + (w >> 1) * HALF + WX + ((w & 1) * 9 + 8) * 1024, n + 8 * hh, 7);
      }
    }
    SB();
    ko = vo == 4 * SLOT ? 0 : vo + SLOT;
    if (n == 0) { STAMPAT(0, 2); }
  }
  STAMPAT(0, 3);
  const rsrc_t rg = mk_rsrc(ows + ((size_t)(br * NB + b) * 9 * NH + kq * 8) * 4096, (9 * NH - kq * 8) * 4096);
  const unsigned go = (2 * hl + hh) * 4096 + lane * 16;
  const unsigned mb = lb + 47104;
  const float l8 = gsum(ls[2]);
#pragma unroll
  for (int t = 0; t < 8; ++t) *(LDS_AS f32x4*)(uintptr_t)(mb + w * 8192 + t * 1024 + lane * 16) = oa[2][t];
  *(LDS_AS f32x2*)(uintptr_t)(mb + 32768 + w * 512 + lane * 8) = f32x2{nm[2], l8};
  u32x4 of[2][4], o8;
#pragma unroll
  for (int a = 0; a < 2; ++a) {
    const float inv = __builtin_amdgcn_rcpf(gsum(ls[a]));
#pragma unroll
    for (int s = 0; s < 4; ++s) {
      of[a][s] = __builtin_bit_cast(u32x4, pack8(oa[a][2 * s] * inv, oa[a][2 * s + 1] * inv));
      __builtin_amdgcn_raw_buffer_store_b128(of[a][s], rg, go + s * 1024, (2 * w + a) * NH * 4096, 16);
    }
  }
  wait_bar<8>();
  {
    f32x2 ml[4];
#pragma unroll
    for (int i = 0; i < 4; ++i) ml[i] = *(const LDS_AS f32x2*)(uintptr_t)(mb + 32768 + i * 512 + lane * 8);
    const float nmin = fminf(fminf(ml[0][0], ml[1][0]), fminf(ml[2][0], ml[3][0]));
    float sc[4], l = 0.f;
#pragma unroll
    for (int i = 0; i < 4; ++i) { sc[i] = __builtin_amdgcn_exp2f(nmin - ml[i][0]); l += sc[i] * ml[i][1]; }
    const float inv = __builtin_amdgcn_rcpf(l);
    f32x4 o0 = {0, 0, 0, 0}, o1 = {0, 0, 0, 0};
#pragma unroll
    for (int i = 0; i < 4; ++i) {
      o0 += *(const LDS_AS f32x4*)(uintptr_t)(mb + i * 8192 + (2 * w) * 1024 + lane * 16) * (sc[i] * inv);
      o1 += *(const LDS_AS f32x4*)(uintptr_t)(mb + i * 8192 + (2 * w + 1) * 1024 + lane * 16) * (sc[i] * inv);
    }
    o8 = __builtin_bit_cast(u32x4, pack8(o0, o1));
    __builtin_amdgcn_raw_buffer_store_b128(o8, rg, go, 8 * NH * 4096 + w * 1024, 16);
  }
  const unsigned ol = lb + w * 4096 + lane * 16, o8l = lb + 16384 + (w & 1) * 1024 + lane * 16;
#pragma unroll
  for (int i = 0; i < 4; ++i) *(LDS_AS u32x4*)(uintptr_t)(ol + i * 1024) = of[i >> 1][i & 1];
  if (w < 2) *(LDS_AS u32x4*)(uintptr_t)o8l = o8;
  asm volatile("s_waitcnt lgkmcnt(0)\n\ts_barrier" ::: "memory");
  STAMPAT(0, 4);
#pragma unroll
  for (int i = 0; i < 4; ++i) *(LDS_AS u32x4*)(uintptr_t)(ol + OY + i * 1024) = of[i >> 1][2 + (i & 1)];
  if (w >= 2) *(LDS_AS u32x4*)(uintptr_t)(o8l + OY) = o8;

  if (hh) {
    const bool iw = w >= 2;
    unsigned vo[9];
#pragma unroll
    for (int i = 0; i < 9; ++i) {
      const int pc = (w & 1) * 9 + i, c = pc * 64 + lane;
      vo[i] = iw ? (c / 18) * (DM * 2) + (c % 18) * 16 : (pc >> 1) * NH * 4096 + (pc & 1) * 1024 + lane * 16;
    }
    auto issue = [&](int t) {
      const unsigned u = ((hl + (t >> 2)) & 3) * 4 + (t & 1) * 2 + ((t >> 1) & 1), sb = lx + (t & 1) * HALF + (w & 1) * 9216;
      if (iw) {
#pragma unroll
        for (int i = 0; i < 9; ++i) dma16<0>(ro, vo[i], u * (64 * DM * 2), sb + WX + (t & 2) * (WX / 2) + i * 1024);
      } else {
#pragma unroll
        for (int i = 0; i < 9; ++i) dma16<2>(rg, vo[i], u * 2048, sb + (t & 2) * (OY / 2) + i * 1024);
      }
    };
    const int peer = (lane & 4) ? (blockIdx.x & ~3u) + (lane & 3) : (blockIdx.x & 63) + 64 * (lane & 3);
    unsigned* const mine = flags + blockIdx.x * 288 + (lane & 3) * 32;
    unsigned e = 0;
    if (iw) { issue(2); issue(3); }
    else e = __hip_atomic_load(flags + peer * 288 + 256, __ATOMIC_RELAXED, __HIP_MEMORY_SCOPE_AGENT) + TAG;
    const unsigned em = __builtin_amdgcn_readlane(e, hl);
    wait_bar<63>(); wait_bar<0>();
    if (!iw) {
      if (w == 0 && lane < 4) __hip_atomic_store(flags + peer * 288 + hl * 32, e, __ATOMIC_RELAXED, __HIP_MEMORY_SCOPE_AGENT);
      while (__any(__hip_atomic_load(mine, __ATOMIC_RELAXED, __HIP_MEMORY_SCOPE_AGENT) != em)) __builtin_amdgcn_s_sleep(1);
      asm volatile("" ::: "memory");
    }
    issue(4); issue(5);
#pragma clang loop unroll(disable)
    for (int t = 6; t < 16; ++t) { wait_bar<18>(); issue(t); }
    wait_bar<18>(); wait_bar<9>(); wait_bar<0>(); wait_bar<0>();
    asm volatile("s_barrier" ::: "memory");
    if (w == 0) {
      if ((lane >> 2) == 1) __hip_atomic_store(flags + peer * 288 + (4 + kq) * 32, e, __ATOMIC_RELAXED, __HIP_MEMORY_SCOPE_AGENT);
      while (__any(__hip_atomic_load(mine + 128, __ATOMIC_RELAXED, __HIP_MEMORY_SCOPE_AGENT) != em)) __builtin_amdgcn_s_sleep(1);
      asm volatile("s_barrier" ::: "memory");
      if (lane == 0) __hip_atomic_store(mine + 256, em, __ATOMIC_RELAXED, __HIP_MEMORY_SCOPE_AGENT);
    } else asm volatile("s_barrier" ::: "memory");
    return;
  }
  const rsrc_t rs = mk_rsrc(xch + (size_t)(blockIdx.x >> 2) * 176 * 1024, 176 * 1024);
  u32x2* po = out + (b * SQ * 2 * DM + br * DM + hl * 144) / 4;
  if (w == 0) oproj_body<0, 0, 5, 0, 4, true>(lx, lane, kq, rs, po STAMP_FWD);
  else if (w == 1) oproj_body<1, 0, 4, 4, 5, false>(lx, lane, kq, rs, po STAMP_FWD);
  else if (w == 2) oproj_body<2, 4, 5, 5, 4, false>(lx, lane, kq, rs, po STAMP_FWD);
  else oproj_body<3, 5, 4, 0, 5, false>(lx, lane, kq, rs, po STAMP_FWD);
  STAMPAT(1, 2);
}

extern "C" void kernel_launch(void* const* d_in, const int* in_sizes, int n_in, void* d_out, int out_size, void* d_ws, size_t ws_size, hipStream_t stream) {
  Ptrs pa = {d_in[0], d_in[1], d_in[2], d_in[3], d_in[4]}, pb = {d_in[5], d_in[6], d_in[7], d_in[8], d_in[9]};
  char* ws = (char*)d_ws;
  fused_kernel<<<256, 512, 0, stream>>>(pa, pb, ws, ws + (32u << 20), (unsigned*)(ws + (64u << 20)), (u32x2*)d_out STAMP_PASS);
}
```

```cpp
#include <hip/hip_runtime.h>
#include <hip/hip_bf16.h>
#include <stdint.h>

typedef __attribute__((ext_vector_type(8))) __bf16 bf16x8;
typedef __attribute__((ext_vector_type(4))) __bf16 bf16x4;
typedef __attribute__((ext_vector_type(2))) __bf16 bf16x2;
typedef __attribute__((ext_vector_type(4))) float f32x4;
typedef __attribute__((ext_vector_type(2))) float f32x2;
typedef __attribute__((ext_vector_type(4))) unsigned u32x4;
typedef __attribute__((ext_vector_type(2))) unsigned u32x2;
#define LDS_AS __attribute__((address_space(3)))
typedef __amdgpu_buffer_rsrc_t rsrc_t;

constexpr int NB = 8, SQ = 144, DM = 576, NH = 32, SK = 512, DH = 128, HD = NH * DH;

#define STAMPAT(kern, idx)
#define STAMP_ARG
#define STAMP_PASS
#define STAMP_FWD

__device__ __forceinline__ rsrc_t mk_rsrc(const void* p, int bytes) {
  return __builtin_amdgcn_make_buffer_rsrc((void*)p, (short)0, bytes, 0x00020000);
}
template <int NT>
__device__ __forceinline__ void dma16(rsrc_t r, unsigned voff, unsigned soff, unsigned ldsb) {
  if constexpr (NT == 2)
    asm volatile("s_mov_b32 m0, %0\n\ts_nop 0\n\tbuffer_load_dwordx4 %1, %2, %3 offen sc1 lds" ::"s"(ldsb), "v"(voff), "s"(r), "s"(soff) : "memory");
  else if constexpr (NT)
    asm volatile("s_mov_b32 m0, %0\n\ts_nop 0\n\tbuffer_load_dwordx4 %1, %2, %3 offen nt lds" ::"s"(ldsb), "v"(voff), "s"(r), "s"(soff) : "memory");
  else
    asm volatile("s_mov_b32 m0, %0\n\ts_nop 0\n\tbuffer_load_dwordx4 %1, %2, %3 offen lds" ::"s"(ldsb), "v"(voff), "s"(r), "s"(soff) : "memory");
}
template <int NT>
__device__ __forceinline__ void dma4(rsrc_t r, unsigned voff, unsigned soff, unsigned ldsb) {
  if constexpr (NT)
    asm volatile("s_mov_b32 m0, %0\n\ts_nop 0\n\tbuffer_load_dword %1, %2, %3 offen nt lds" ::"s"(ldsb), "v"(voff), "s"(r), "s"(soff) : "memory");
  else
    asm volatile("s_mov_b32 m0, %0\n\ts_nop 0\n\tbuffer_load_dword %1, %2, %3 offen lds" ::"s"(ldsb), "v"(voff), "s"(r), "s"(soff) : "memory");
}
template <int NT>
__device__ __forceinline__ void dma16_if(rsrc_t r, unsigned voff, unsigned soff, unsigned ldsb, int a, int b) {
  if constexpr (NT)
    asm volatile("s_cmp_lg_u32 %4, %5\n\ts_cbranch_scc1 .Lsk%=\n\ts_mov_b32 m0, %0\n\ts_nop 0\n\tbuffer_load_dwordx4 %1, %2, %3 offen nt lds\n.Lsk%=:" ::"s"(ldsb), "v"(voff), "s"(r), "s"(soff), "s"(a), "s"(b) : "memory", "scc");
  else
    asm volatile("s_cmp_lg_u32 %4, %5\n\ts_cbranch_scc1 .Lsk%=\n\ts_mov_b32 m0, %0\n\ts_nop 0\n\tbuffer_load_dwordx4 %1, %2, %3 offen lds\n.Lsk%=:" ::"s"(ldsb), "v"(voff), "s"(r), "s"(soff), "s"(a), "s"(b) : "memory", "scc");
}
__device__ __forceinline__ void dma16nt_lt(rsrc_t r, unsigned voff, unsigned soff, unsigned ldsb, int add, int a, int b) {
  unsigned t;
  asm volatile("s_cmp_ge_u32 %5, %6\n\ts_cbranch_scc1 .Lsk%=\n\ts_add_u32 m0, %1, %7\n\ts_add_u32 %0, %4, %7\n\tbuffer_load_dwordx4 %2, %3, %0 offen nt lds\n.Lsk%=:" : "=&s"(t) : "s"(ldsb), "v"(voff), "s"(r), "s"(soff), "s"(a), "i"(b), "i"(add) : "memory", "scc");
}
template <int NT>
__device__ __forceinline__ void dma4_eq(rsrc_t r, unsigned voff, unsigned soff, unsigned ldsb, int a, int b) {
  if constexpr (NT)
    asm volatile("s_cmp_lg_u32 %3, %4\n\ts_cbranch_scc1 .Lsk%=\n\ts_mov_b32 m0, %0\n\ts_nop 0\n\tbuffer_load_dword %1, %2, %5 offen nt lds\n.Lsk%=:" ::"s"(ldsb), "v"(voff), "s"(r), "s"(a), "i"(b), "s"(soff) : "memory", "scc");
  else
    asm volatile("s_cmp_lg_u32 %3, %4\n\ts_cbranch_scc1 .Lsk%=\n\ts_mov_b32 m0, %0\n\ts_nop 0\n\tbuffer_load_dword %1, %2, %5 offen lds\n.Lsk%=:" ::"s"(ldsb), "v"(voff), "s"(r), "s"(a), "i"(b), "s"(soff) : "memory", "scc");
}
#define SB() __builtin_amdgcn_sched_barrier(0)
template <int N>
__device__ __forceinline__ void wait_bar() {
  asm volatile("s_waitcnt vmcnt(%0) lgkmcnt(0)\n\ts_barrier" ::"i"(N) : "memory");
}
template <int N0, int N>
__device__ __forceinline__ void wait_bar2(int a, int b) {
  asm volatile("s_cmp_eq_u32 %2, %3\n\ts_cbranch_scc1 .Lw2%=\n\ts_waitcnt vmcnt(%1)\n.Lw2%=:\n\ts_waitcnt vmcnt(%0) lgkmcnt(0)\n\ts_barrier" ::"i"(N0), "i"(N), "s"(a), "s"(b) : "memory", "scc");
}
__device__ __forceinline__ bf16x8 lds_b128(unsigned a) { return *(const LDS_AS bf16x8*)(uintptr_t)a; }
__device__ __forceinline__ bf16x4 lds_tr(unsigned a) { return __builtin_amdgcn_ds_read_tr16_b64_v4bf16((LDS_AS bf16x4*)(uintptr_t)a); }
__device__ __forceinline__ bf16x8 lds_tr2(unsigned a0, unsigned a1) {
  bf16x4 x = lds_tr(a0), y = lds_tr(a1);
  return __builtin_shufflevector(x, y, 0, 1, 2, 3, 4, 5, 6, 7);
}
__device__ __forceinline__ f32x4 mfma(bf16x8 a, bf16x8 b, f32x4 c) { return __builtin_amdgcn_mfma_f32_16x16x32_bf16(a, b, c, 0, 0, 0); }
__device__ __forceinline__ void mfma_p(bf16x8 a, bf16x8 b, f32x4& c) { c = mfma(a, b, c); asm volatile("" : "+v"(c)); }
__device__ __forceinline__ unsigned pk2(float a, float b) {
  f32x2 v = {a, b};
  bf16x2 r = __builtin_convertvector(v, bf16x2);
  return __builtin_bit_cast(unsigned, r);
}
__device__ __forceinline__ bf16x8 pack8(f32x4 a, f32x4 b) {
  u32x4 w = {pk2(a[0], a[1]), pk2(a[2], a[3]), pk2(b[0], b[1]), pk2(b[2], b[3])};
  return __builtin_bit_cast(bf16x8, w);
}
__device__ __forceinline__ float fmx(float a, float b) { return __builtin_elementwise_maximum(a, b); }
__device__ __forceinline__ float gmax(float v) {
  auto a = __builtin_amdgcn_permlane16_swap(__float_as_uint(v), __float_as_uint(v), false, false);
  v = fmx(__uint_as_float(a[0]), __uint_as_float(a[1]));
  auto b = __builtin_amdgcn_permlane32_swap(__float_as_uint(v), __float_as_uint(v), false, false);
  return fmx(__uint_as_float(b[0]), __uint_as_float(b[1]));
}
__device__ __forceinline__ float gsum(float v) {
  auto a = __builtin_amdgcn_permlane16_swap(__float_as_uint(v), __float_as_uint(v), false, false);
  v = __uint_as_float(a[0]) + __uint_as_float(a[1]);
  auto b = __builtin_amdgcn_permlane32_swap(__float_as_uint(v), __float_as_uint(v), false, false);
  return __uint_as_float(b[0]) + __uint_as_float(b[1]);
}
__device__ __forceinline__ float max4(f32x4 a) { return fmx(fmx(a[0], a[1]), fmx(a[2], a[3])); }

#define NT_KV 1
#define PP 1
#define GSL SLOT
#define BURST 0
#define LIM 6
#define EXPF __builtin_amdgcn_exp2f
#define CODE 49152
#define KS 0
#define VS0 1
#define WE 0
#define PRI 1
#define NTI 1
#define NTO 0
#define WN 6
#define KPX (((b + hl) & 1) * 4)
#define GA 0
#define GD 3
#ifndef NV
#define NV 1
#endif
#define WG0 6
#define NT_W 0
constexpr float QSCALE = 0.08838834764831845f * 1.4426950408889634f;
constexpr float PLIM = 4096.f;
constexpr float PMIN = 1.f / 4096.f;
constexpr int SLOT = 16384, HALF = 81920, XB0 = 49152, XB1 = HALF + XB0, Q8OFF = 77824;
__device__ __forceinline__ constexpr int wbuf(int i) { return i == 0 ? 32768 : i == 1 ? 40960 : 67584; }

struct Ptrs { const void *x, *k, *v, *wq, *wo; };

constexpr int WX = 18432, OY = 3 * WX;
constexpr unsigned TAG = 0x9E3779B9u;
template <int W, int Q0, int NQ, int N0, int NN, bool CEN>
__device__ __forceinline__ void oproj_body(unsigned lb, int lane, int kq, rsrc_t rx, u32x2* __restrict__ out STAMP_ARG) {
  const int c16 = lane & 15, g = lane >> 4;
  constexpr int NW = NN + CEN, NM = NQ * NN + CEN;
  f32x4 acc[NM];
#pragma unroll
  for (int m = 0; m < NM; ++m) acc[m] = f32x4{0, 0, 0, 0};
  const unsigned ao0 = lb + Q0 * 2048 + lane * 16;
  const unsigned aw0 = lb + 18432 + (4 * g + (c16 >> 2)) * 288 + N0 * 32 + 8 * (c16 & 3);
  unsigned ao = ao0, aw = aw0;
  bf16x8 of[2][NQ]; bf16x4 wf[2][NW][2];
  auto rd = [&](int ks, int i) {
    const int f = i / 3, r = i % 3;
    if (r == 2) { if (f < NQ) of[ks][f] = lds_b128(ao + f * 2048 + ks * 1024); }
    else if (f < NW) wf[ks][f][r] = lds_tr(aw + f * 32 + (ks * 32 + 16 * r) * 288);
  };
  auto half = [&](int ks) {
#pragma unroll
    for (int m = 0; m < NM; ++m) {
      SB();
      const int t = m < NQ * NN ? m / NQ : NW - 1, a = m < NQ * NN ? m % NQ : NQ - 1;
      acc[m] = mfma(__builtin_shufflevector(wf[ks][t][0], wf[ks][t][1], 0, 1, 2, 3, 4, 5, 6, 7), of[ks][a], acc[m]);
      SB();
      if (m < 15) rd(ks ^ 1, m);
    }
    SB();
  };
  STAMPAT(1, 3);
#pragma unroll
  for (int i = 0; i < 15; ++i) rd(0, i);
#pragma clang loop unroll(disable)
  for (int t = 1; t <= 16; ++t) {
    half(0);
    ao = ao0 + ((t & 1) * HALF + (t & 2) * (OY / 2)); aw = aw0 + ((t & 1) * HALF + (t & 2) * (WX / 2));
    asm volatile("s_cmp_lg_u32 %2, 2\n\ts_cbranch_scc1 .Lo%=\n\ts_waitcnt vmcnt(0)\n.Lo%=:\n\ts_waitcnt lgkmcnt(0)\n\ts_barrier" : "+v"(ao), "+v"(aw) : "s"(t) : "memory", "scc");
    half(1);
  }
  STAMPAT(1, 1);
  constexpr int NP = (NM + 1) / 2;
  auto fin = [&](auto kc) {
    constexpr int K = decltype(kc)::value;
#pragma unroll
    for (int p = 0; p < NP; ++p)
      if (((p + 2 * W) & 3) != K) __builtin_amdgcn_raw_buffer_store_b128(__builtin_bit_cast(u32x4, pack8(acc[2 * p], acc[2 * p + 1 < NM ? 2 * p + 1 : 2 * p])), rx, lane * 16, ((K * 4 + W) * 11 + p) * 1024, 16);
    asm volatile("s_waitcnt vmcnt(0)\n\ts_barrier" ::: "memory");
    STAMPAT(1, 4);
    asm volatile("s_barrier" ::: "memory");
    STAMPAT(1, 5);
    u32x4 in[3][3];
#pragma unroll
    for (int p = 0; p < NP; ++p)
      if (((p + 2 * W) & 3) == K) {
#pragma unroll
        for (int i = 0; i < 3; ++i) in[p >> 2][i] = __builtin_amdgcn_raw_buffer_load_b128(rx, lane * 16, ((((K + 1 + i) & 3) * 4 + W) * 11 + p) * 1024, 16);
      }
#pragma unroll
    for (int m = 0; m < NM; ++m)
      if ((((m >> 1) + 2 * W) & 3) == K) {
        f32x4 v = acc[m];
#pragma unroll
        for (int i = 0; i < 3; ++i) {
          const unsigned u0 = in[m >> 3][i][2 * (m & 1)], u1 = in[m >> 3][i][2 * (m & 1) + 1];
          v += f32x4{__uint_as_float(u0 << 16), __uint_as_float(u0 & 0xffff0000u), __uint_as_float(u1 << 16), __uint_as_float(u1 & 0xffff0000u)};
        }
        const int qt = m < NQ * NN ? Q0 + m % NQ : 4, nt = m < NQ * NN ? N0 + m / NQ : 4;
        out[(qt * 16 + c16) * (2 * DM / 4) + nt * 4 + g] = u32x2{pk2(v[0], v[1]), pk2(v[2], v[3])};
      }
  };
  using std::integral_constant;
  if (kq == 0) fin(integral_constant<int, 0>{}); else if (kq == 1) fin(integral_constant<int, 1>{});
  else if (kq == 2) fin(integral_constant<int, 2>{}); else fin(integral_constant<int, 3>{});
}
__global__ __launch_bounds__(512) void fused_kernel(Ptrs pa, Ptrs pb, char* ows, char* xch, unsigned* flags, u32x2* __restrict__ out STAMP_ARG) {
  __shared__ __attribute__((aligned(16))) char smem[2 * HALF];
  const int lane = threadIdx.x & 63, w8 = __builtin_amdgcn_readfirstlane(threadIdx.x >> 6), w = w8 & 3, hh = w8 >> 2;
  const unsigned lx = (unsigned)(uintptr_t)(LDS_AS char*)smem, lb = lx + hh * HALF;
  STAMPAT(0, 0);
  const int c16 = lane & 15, g = lane >> 4;
  const int xcd = blockIdx.x & 7, j = blockIdx.x >> 3;
  const int br = xcd >> 2, b = j & 7, hl = j >> 3, h = (xcd & 3) * 8 + 2 * hl + hh;
  const Ptrs p = br ? pb : pa;
  const rsrc_t rx = mk_rsrc((const char*)p.x + (size_t)b * SQ * DM * 2, SQ * DM * 2);
  const rsrc_t rw = mk_rsrc((const char*)p.wq + h * DH * 2, DM * HD * 2 - h * DH * 2);
  const rsrc_t rk = mk_rsrc((const char*)p.k + (size_t)(b * NH + h) * SK * DH * 2, SK * DH * 2);
  const rsrc_t rv = mk_rsrc((const char*)p.v + (size_t)(b * NH + h) * SK * DH * 2, SK * DH * 2);

  const unsigned fw = (g & 1) | ((g & 2) << 1) | ((w & 1) << 3);
  const unsigned vo_w = (8 * w + g) * (HD * 2) + ((c16 ^ fw) << 4);
  const unsigned vo_x0 = (lane >> 3) * (DM * 2) + (((lane & 7) ^ g) << 4), vo_x1 = (vo_x0 ^ 64) + 8 * DM * 2;
  const int r8 = (lane >> 2) & 7;
  const unsigned vo_kv = (8 * (w >> 1) + r8) * 256 + (2 * (w & 1) + (lane >> 5)) * 64;
  const unsigned vo_k = vo_kv + (((lane & 3) ^ (w & 2)) << 4);
  const unsigned vo_v = vo_kv + (((lane & 3) ^ ((r8 & 4) >> 1)) << 4);

  auto iss_w = [&](int kc, int buf, int i) { dma16<NT_W>(rw, vo_w, (kc * 32 + 4 * i) * HD * 2, lb + wbuf(buf) + w * 2048 + i * 1024); };
  auto iss_x = [&](int kp, unsigned xb, int i) { dma16<NT_W>(rx, i ? vo_x1 : vo_x0, kp * 128 + w8 * 16 * DM * 2, xb + w8 * 2048 + i * 1024); };
  auto iss_x8 = [&](int kp, unsigned xb, int i, int who) { dma16_if<NT_W>(rx, i ? vo_x1 : vo_x0, kp * 128 + 128 * DM * 2, xb + 16384 + i * 1024, w8, who); };
  auto iss_kv = [&](int t, int i) {
    const unsigned sb = lb + (t % 5) * SLOT + w * 1024, so = (t >> 1) * SLOT;
    if (t & 1) dma16<NT_KV>(rv, vo_v, so + i * 4096, sb + i * 4096);
    else dma16<NT_KV>(rk, vo_k, so + i * 4096, sb + i * 4096);
  };

  int kp = KPX; kp = kp >= 9 ? kp - 9 : kp;
  int kc = 2 * kp;
  auto nkc = [&]() { kc = kc == 17 ? 0 : kc + 1; };
  auto nkp = [&]() { kp = kp == 8 ? 0 : kp + 1; };
  auto ini_w = [&](int kc, int buf, int i) { dma16<NTI & 1>(rw, vo_w, (kc * 32 + 4 * i) * HD * 2, lb + wbuf(buf) + w * 2048 + i * 1024); };
  auto ini_x = [&](int kp, unsigned xb, int i) { dma16<(NTI >> 1)>(rx, i ? vo_x1 : vo_x0, kp * 128 + w8 * 16 * DM * 2, xb + w8 * 2048 + i * 1024); };
  iss_x8(kp, lx + XB0, 0, 0); iss_x8(kp, lx + XB0, 1, 1);
  ini_x(kp, lx + XB0, 0); ini_x(kp, lx + XB0, 1); nkp();
  ini_w(kc, 0, 0); ini_w(kc, 0, 1); nkc();
  asm volatile("s_barrier" ::: "memory");
  iss_x8(kp, lx + XB1, 0, 2); iss_x8(kp, lx + XB1, 1, 3);
  ini_w(kc, 1, 0); ini_w(kc, 1, 1); nkc(); ini_x(kp, lx + XB1, 0); ini_x(kp, lx + XB1, 1); nkp();
  ini_w(kc, 2, 0); ini_w(kc, 2, 1); nkc();
  {
    unsigned long long pc; asm volatile("s_getpc_b64 %0" : "=s"(pc));
    dma4<0>(mk_rsrc((const char*)(pc & ~127ull), w8 == 7 ? CODE : 0), (j & 7) * 8192 + lane * 128, 0, lb + 75776 + w * 256);
  }
  f32x4 qa[2][8], q8[2];
#pragma unroll
  for (int t = 0; t < 8; ++t) { qa[0][t] = f32x4{0, 0, 0, 0}; qa[1][t] = f32x4{0, 0, 0, 0}; }
  q8[0] = f32x4{0, 0, 0, 0}; q8[1] = f32x4{0, 0, 0, 0};
  unsigned aw[8];
  {
    const int qq = c16 >> 2, pp = c16 & 3;
    const unsigned f = (qq & 1) | ((qq & 2) << 1) | ((g & 1) << 3);
#pragma unroll
    for (int s = 0; s < 4; ++s) { aw[2 * s] = lb + wbuf(0) + (8 * g + qq) * 256 + ((((unsigned)(4 * (s ^ w) + pp)) ^ f) << 4) + 8 * (pp & 1); aw[2 * s + 1] = aw[2 * s] ^ 8; }
  }
  const unsigned axl = c16 * 128 + ((g ^ (c16 >> 1)) << 4);
  unsigned xrd = lx + XB0, xwr = lx + XB0;
  bf16x8 fx[2][3], fy[3]; bf16x4 fq[2][8][2];
  unsigned ax[4];
  auto x_addr = [&]() { ax[0] = axl + (xrd + w * 4096); ax[1] = ax[0] ^ 64; ax[2] = axl + (xrd + 16384); ax[3] = ax[2] ^ 64; xrd = 2 * lx + XB0 + XB1 - xrd; };
  auto rd_w = [&](int set, int buf, int i) { fq[set][i >> 1][i & 1] = lds_tr(aw[i >> 1] + (wbuf(buf) - wbuf(0)) + (i & 1) * 1024); };
  auto rd_x = [&](int i) {
    const bf16x8 v = lds_b128(i % 3 == 2 ? ax[2 + i / 3] : ax[i / 3] + (i % 3) * 2048);
    if (i < 3) fx[0][i] = v; else fy[i - 3] = v;
  };
  x_addr();
  wait_bar<7>();
  STAMPAT(0, 5);
#pragma unroll
  for (int i = 0; i < 16; ++i) rd_w(0, 0, i);
#pragma unroll
  for (int i = 0; i < 6; ++i) rd_x(i);
  rsrc_t rt = rk; unsigned vt = vo_k, lt = lb + w * 1024;
  const rsrc_t rp = mk_rsrc((const char*)p.wo + ((size_t)h * DH + b * 16) * DM * 2 + w * 4608, 4608);
  asm volatile("s_waitcnt vmcnt(5)" ::: "memory");
  auto q_step = [&](auto uc, auto tailc, int who, int prev) {
    constexpr int u = decltype(uc)::value, cs = u & 1; constexpr bool TAIL = decltype(tailc)::value;
    if (cs) {
#pragma unroll
      for (int i = 0; i < 3; ++i) fx[1][i] = fy[i];
      x_addr();
    }
    constexpr int NF = u == 0 || u == 4 ? 6 : u == 5 ? 5 : 4;
    if (!TAIL || u < 3) wait_bar2<NF + 1, NF>(w8, prev);
    else if (u < 5) wait_bar<u == 3 ? 2 : 0>();
    int gap = 0;
    auto fill = [&]() {
      SB();
      if (!(TAIL && u == 5)) {
        constexpr int G0 = WG0;
        if (gap >= G0 && gap < G0 + 8) { rd_w(cs ^ 1, (u + 1) % 3, 2 * (gap - G0)); rd_w(cs ^ 1, (u + 1) % 3, 2 * (gap - G0) + 1); }
        else if (cs && gap < 14) rd_x(gap < G0 ? gap : gap - 8);
      }
      if ((!TAIL || u < 3) && (gap == GA || gap == GA + GD)) iss_w(kc, u % 3, gap != GA);
      if ((!TAIL || u < 2) && gap == GA + 2 * GD) iss_x8(kp, xwr, cs, who);
      if ((!TAIL || u < 2) && gap == GA + 3 * GD) iss_x(kp, xwr, cs);
      if (!TAIL && u >= 2 && gap == GA + 4 * GD) dma16<NT_KV>(rt, vt, (u - 2) * 4096, lt + (u - 2) * 4096);
      if (TAIL && u > 3 && gap >= 2 && gap < 14 && gap % 3 == 2) iss_kv(u - 2, (gap - 2) / 3);
      ++gap;
      SB();
    };
#pragma unroll
    for (int f = 0; f < 8; ++f) {
      const bf16x8 wf = __builtin_shufflevector(fq[cs][f][0], fq[cs][f][1], 0, 1, 2, 3, 4, 5, 6, 7);
      mfma_p(wf, fx[cs][0], qa[0][f]); fill();
      mfma_p(wf, fx[cs][1], qa[1][f]); fill();
      if (f < 2) { mfma_p(wf, fx[cs][2], q8[f]); fill(); }
    }
    nkc();
    if (cs) { nkp(); xwr = 2 * lx + XB0 + XB1 - xwr; }
  };
  using std::integral_constant;
#define QS(u, tl, who, prev) q_step(integral_constant<int, u>{}, integral_constant<bool, tl>{}, who, prev)
#pragma clang loop unroll(disable)
  for (int it = 0; it < 8; it += 4) { QS(0, false, it, 15 - 2 * it); QS(1, false, it + 1, it); QS(2, false, it + 2, it + 1); QS(3, false, it + 3, it + 2); QS(4, false, it ^ 6, it + 3); QS(5, false, it ^ 7, it ^ 6); rt = rv; vt = vo_v; lt += SLOT; }
  STAMPAT(0, 6);
  QS(0, true, 0, 3); QS(1, true, 1, 0); QS(2, true, 0, 1); QS(3, true, 0, 0); STAMPAT(0, 7); QS(4, true, 0, 0); QS(5, true, 0, 0);
  STAMPAT(0, 1);
  bf16x8 qf[3][4];
  auto qpack = [&](f32x4 lo, f32x4 hi) {
    lo *= QSCALE; hi *= QSCALE;
    const unsigned a0 = pk2(lo[0], lo[1]), a1 = pk2(lo[2], lo[3]), b0 = pk2(hi[0], hi[1]), b1 = pk2(hi[2], hi[3]);
    const bool odd = g & 1;
    u32x4 r = {odd ? b0 : a0, odd ? b1 : a1, odd ? a0 : b0, odd ? a1 : b1};
    return __builtin_bit_cast(bf16x8, r);
  };
  *(LDS_AS bf16x8*)(uintptr_t)(lb + Q8OFF + w * 1024 + lane * 16) = qpack(q8[0], q8[1]);
  wait_bar<8>();
#pragma unroll
  for (int s = 0; s < 4; ++s) qf[2][s] = lds_b128(lb + Q8OFF + s * 1024 + lane * 16);
  if (PP && hh) asm volatile("s_barrier" ::: "memory");
#pragma unroll
  for (int t = 0; t < 8; ++t) asm volatile("" : "+v"(qa[0][t]), "+v"(qa[1][t]));
#pragma unroll
  for (int s = 0; s < 4; ++s) { qf[0][s] = qpack(qa[0][2 * s], qa[0][2 * s + 1]); qf[1][s] = qpack(qa[1][2 * s], qa[1][2 * s + 1]); }
#pragma unroll
  for (int a = 0; a < 2; ++a) {
    if (w & 1) { bf16x8 t = qf[a][0]; qf[a][0] = qf[a][1]; qf[a][1] = t; t = qf[a][2]; qf[a][2] = qf[a][3]; qf[a][3] = t; }
    if (w & 2) { bf16x8 t = qf[a][0]; qf[a][0] = qf[a][2]; qf[a][2] = t; t = qf[a][1]; qf[a][1] = qf[a][3]; qf[a][3] = t; }
  }
  f32x4 oa[3][8];
#pragma unroll
  for (int t = 0; t < 8; ++t) { oa[0][t] = f32x4{0, 0, 0, 0}; oa[1][t] = f32x4{0, 0, 0, 0}; oa[2][t] = f32x4{0, 0, 0, 0}; }
  float ls[3] = {0.f, 0.f, 0.f};
  f32x4 nv[3] = {{0, 0, 0, 0}, {0, 0, 0, 0}, {0, 0, 0, 0}};
  const unsigned ak = lb + (c16 >> 3) * 2048 + (c16 & 7) * 64 + ((g ^ ((c16 & 8) >> 2)) << 4);
  const unsigned av = lb + (g >> 1) * 2048 + (4 * (g & 1) + (c16 >> 2)) * 64 + (g & 1) * 32 + 8 * (c16 & 3);
  const int kq = xcd & 3;
  const rsrc_t ro = mk_rsrc((const char*)p.wo + (size_t)kq * 1024 * DM * 2 + hl * 288, 1024 * DM * 2 - hl * 288);
  const unsigned vo_a = ((w8 * 64 + lane) / 18) * (DM * 2) + ((w8 * 64 + lane) % 18) * 16, vo_b = ((512 + lane) / 18) * (DM * 2) + ((512 + lane) % 18) * 16;
  unsigned ko = 0;
#pragma clang loop unroll(disable)
  for (int n0 = 0; n0 < 8; ++n0) {
    int n = n0; asm volatile("" : "+s"(n));
    const unsigned vo = ko == 4 * SLOT ? 0 : ko + SLOT, po = ko == 0 ? 4 * SLOT : ko - SLOT;
    unsigned kb[4];
#pragma unroll
    for (int kt = 0; kt < 4; ++kt) kb[kt] = ak + (ko + (kt ^ w) * 4096);
    asm volatile("s_cmp_lt_u32 %0, 7\n\ts_cbranch_scc1 .Lwa%=\n\ts_waitcnt vmcnt(5)\n.Lwa%=:\n\ts_waitcnt vmcnt(12) lgkmcnt(0)\n\ts_barrier" ::"s"(n) : "memory", "scc");
    if (PRI) __builtin_amdgcn_s_setprio(PRI & 1);
    bf16x8 kf[2][4];
#pragma unroll
    for (int s = 0; s < 4; ++s) kf[0][s] = lds_b128(kb[0] + s * 512);
    SB();
    unsigned gso = (n + 2) * GSL, kld = lb + po + w * 1024, vld = lb + ko + w * 1024;
    asm volatile("" : "+s"(gso), "+s"(kld), "+s"(vld));
    auto iss_k = [&](int i, int lim) { dma16nt_lt(rk, vo_k, gso, kld, i * 4096, n, lim); };
    f32x4 sa[2][4], s8;
    float ts[3];
    bf16x8 pf[2][2], p8;
    auto ex = [&](f32x4& v, float& t, bool init) {
#pragma unroll
      for (int r = 0; r < 4; ++r) { v[r] = EXPF(v[r]); if (init && r == 0) t = v[0]; else t += v[r]; }
    };
    auto packs = [&]() {
      pf[0][0] = pack8(sa[0][0], sa[0][1]); pf[1][0] = pack8(sa[1][0], sa[1][1]); pf[0][1] = pack8(sa[0][2], sa[0][3]); pf[1][1] = pack8(sa[1][2], sa[1][3]);
      u32x4 t = {pk2(s8[0], s8[1]), pk2(s8[2], s8[3]), 0u, 0u}; p8 = __builtin_bit_cast(bf16x8, t);
    };
    bool ok;
    {
#pragma unroll
      for (int kt = 0; kt < 4; ++kt) {
        sa[0][kt] = nv[0]; sa[1][kt] = nv[1];
        if (kt == 0) s8 = nv[2];
#pragma unroll
        for (int s = 0; s < 4; ++s) {
          SB();
          sa[0][kt] = mfma(kf[kt & 1][s], qf[0][s], sa[0][kt]);
          if (kt < 3) {
 kf[(kt & 1) ^ 1][s] = lds_b128(kb[kt + 1] + s * 512);
}
          sa[1][kt] = mfma(kf[kt & 1][s], qf[1][s], sa[1][kt]);
          if (kt == 0) s8 = mfma(kf[0][s], qf[2][s], s8);
          if (BURST ? kt == KS : s == KS) { SB(); iss_k(BURST ? s : kt, LIM); if ((BURST ? s : kt) == 0) dma4_eq<NTO>(rp, lane * 128, 0, lb + SLOT + w * 256, n, 6); }
          if (kt > 0 && s == 1) ex(sa[0][kt - 1], ts[0], kt == 1);
          if (kt > 0 && s == 2) ex(sa[1][kt - 1], ts[1], kt == 1);
          if (kt == 1 && s == 3) ex(s8, ts[2], true);
        }
      }
      SB();
      ex(sa[0][3], ts[0], false); ex(sa[1][3], ts[1], false);
      packs();
      ok = !__any(!(ts[0] + ts[1] + ts[2] <= PLIM) || !(fminf(fminf(ts[0], ts[1]), ts[2]) >= (n ? 0.f : PMIN)));
    }
    if (__builtin_expect(!ok, 0)) {
      asm volatile("" : "+v"(kb[0]), "+v"(kb[1]), "+v"(kb[2]), "+v"(kb[3]));
#pragma unroll
      for (int kt = 0; kt < 4; ++kt) {
        sa[0][kt] = nv[0]; sa[1][kt] = nv[1];
        if (kt == 0) s8 = nv[2];
#pragma unroll
        for (int s = 0; s < 4; ++s) kf[0][s] = lds_b128(kb[kt] + s * 512);
#pragma unroll
        for (int s = 0; s < 4; ++s) {
          sa[0][kt] = mfma(kf[0][s], qf[0][s], sa[0][kt]);
          sa[1][kt] = mfma(kf[0][s], qf[1][s], sa[1][kt]);
          if (kt == 0) s8 = mfma(kf[0][s], qf[2][s], s8);
        }
      }
      float d0 = gmax(fmx(fmx(max4(sa[0][0]), max4(sa[0][1])), fmx(max4(sa[0][2]), max4(sa[0][3]))));
      float d1 = gmax(fmx(fmx(max4(sa[1][0]), max4(sa[1][1])), fmx(max4(sa[1][2]), max4(sa[1][3]))));
      float d2 = gmax(max4(s8));
      if (n != 0) {
        d0 = fmx(d0, 0.f); d1 = fmx(d1, 0.f); d2 = fmx(d2, 0.f);
        const float a0 = __builtin_amdgcn_exp2f(-d0), a1 = __builtin_amdgcn_exp2f(-d1), a2 = __builtin_amdgcn_exp2f(-d2);
        ls[0] *= a0; ls[1] *= a1; ls[2] *= a2;
#pragma unroll
        for (int t = 0; t < 8; ++t) { oa[0][t] *= a0; oa[1][t] *= a1; oa[2][t] *= a2; }
      }
      nv[0] -= d0; nv[1] -= d1; nv[2] -= d2;
      if (NV) asm volatile("" : "+v"(nv[0]), "+v"(nv[1]), "+v"(nv[2]));
#pragma unroll
      for (int kt = 0; kt < 4; ++kt) { sa[0][kt] -= d0; sa[1][kt] -= d1; ex(sa[0][kt], ts[0], kt == 0); ex(sa[1][kt], ts[1], kt == 0); }
      s8 -= d2; ex(s8, ts[2], true);
      packs();
    }
    ls[0] += ts[0]; ls[1] += ts[1]; ls[2] += ts[2];
    unsigned vb[4];
#pragma unroll
    for (int i = 0; i < 4; ++i) vb[i] = av + (vo + (i ^ w) * 4096);
    asm volatile("s_cmp_lt_u32 %0, 6\n\ts_cbranch_scc1 .Lwb%=\n\ts_waitcnt vmcnt(9)\n\ts_cmp_lt_u32 %0, 7\n\ts_cbranch_scc1 .Lwb%=\n\ts_waitcnt vmcnt(1)\n.Lwb%=:\n\ts_waitcnt vmcnt(12) lgkmcnt(0)\n\ts_barrier" ::"s"(n) : "memory", "scc");
    if (PRI) __builtin_amdgcn_s_setprio(PRI >> 1);
    bf16x8 vf[4];
    auto rd_v = [&](int i) { const int m = i >> 3, t = (i & 3) * 2 + ((i >> 2) & 1); vf[i & 3] = lds_tr2((vb[2 * m] ^ ((t & 1) * 32)) + (t >> 1) * 512, (vb[2 * m + 1] ^ ((t & 1) * 32)) + (t >> 1) * 512); };
    rd_v(0); rd_v(1); rd_v(2);
    SB();
#pragma unroll
    for (int i = 0; i < 16; ++i) {
      const int m = i >> 3, t = (i & 3) * 2 + ((i >> 2) & 1);
      SB();
      oa[0][t] = mfma(vf[i & 3], pf[0][m], oa[0][t]);
      if (i < 13) rd_v(i + 3);
      oa[1][t] = mfma(vf[i & 3], pf[1][m], oa[1][t]);
      if (m == 0) oa[2][t] = mfma(vf[i & 3], p8, oa[2][t]);
      if (BURST ? (i >= VS0 && i < VS0 + 4) : (i == VS0 || i == VS0 + 3 || i == VS0 + 6 || i == VS0 + 10)) { SB(); const int q = (i - VS0) / (BURST ? 1 : 3); dma16nt_lt(rv, vo_v, gso, vld, q * 4096, n, LIM);
        dma16_if<0>(ro, vo_a, (hl * 256 + (q >> 1) * 128 + (q & 1) * 32) * DM * 2, lx + (q >> 1) * HALF + WX + ((q & 1) * 9 + w8) * 1024, n, 7);
        if (q == 3) dma16_if<0>(ro, vo_b, (hl * 256 + (w >> 1) * 128 + (w & 1) * 32) * DM * 2, lx + (w >> 1) * HALF + WX + ((w & 1) * 9 + 8) * 1024, n + 8 * hh, 7);
      }
    }
    SB();
    ko = vo == 4 * SLOT ? 0 : vo + SLOT;
    if (n == 0) { STAMPAT(0, 2); }
  }
  if (PP && !hh) asm volatile("s_barrier" ::: "memory");
  STAMPAT(0, 3);
  const rsrc_t rg = mk_rsrc(ows + ((size_t)(br * NB + b) * 9 * NH + kq * 8) * 4096, (9 * NH - kq * 8) * 4096);
  const unsigned go = (2 * hl + hh) * 4096 + lane * 16;
  const unsigned mb = lb + 47104;
  const float l8 = gsum(ls[2]);
#pragma unroll
  for (int t = 0; t < 8; ++t) *(LDS_AS f32x4*)(uintptr_t)(mb + w * 8192 + t * 1024 + lane * 16) = oa[2][t];
  *(LDS_AS f32x2*)(uintptr_t)(mb + 32768 + w * 512 + lane * 8) = f32x2{nv[2][0], l8};
  u32x4 of[2][4], o8;
#pragma unroll
  for (int a = 0; a < 2; ++a) {
    const float inv = __builtin_amdgcn_rcpf(gsum(ls[a]));
#pragma unroll
    for (int s = 0; s < 4; ++s) {
      of[a][s] = __builtin_bit_cast(u32x4, pack8(oa[a][2 * s] * inv, oa[a][2 * s + 1] * inv));
      __builtin_amdgcn_raw_buffer_store_b128(of[a][s], rg, go + s * 1024, (2 * w + a) * NH * 4096, 16);
    }
  }
  const bool iw = w >= 2;
  unsigned vo[9];
  if (!hh) {
    unsigned r = lane % 18;
    vo[0] = iw ? (lane / 18 + (w & 1) * 32) * (DM * 2) + r * 16 : (w & 1) * (4 * NH * 4096 + 1024) + lane * 16;
#pragma unroll
    for (int i = 1; i < 9; ++i) {
      r += 10;
      const bool c = r >= 18;
      r = c ? r - 18 : r;
      const unsigned so = ((w + i) & 1) ? 1024 : NH * 4096 - 1024, d0 = iw ? 3616 : so, d1 = iw ? 4480 : so;
      vo[i] = vo[i - 1] + (c ? d1 : d0);
    }
#pragma unroll
    for (int i = 0; i < 9; ++i) asm volatile("" : "+v"(vo[i]));
  }
  wait_bar<8>();
  {
    f32x2 ml[4]; f32x4 pr[8];
#pragma unroll
    for (int i = 0; i < 4; ++i) ml[i] = *(const LDS_AS f32x2*)(uintptr_t)(mb + 32768 + i * 512 + lane * 8);
#pragma unroll
    for (int i = 0; i < 8; ++i) pr[i] = *(const LDS_AS f32x4*)(uintptr_t)(mb + w * 2048 + lane * 16 + (i >> 1) * 8192 + (i & 1) * 1024);
    SB();
    const float nmin = fminf(fminf(ml[0][0], ml[1][0]), fminf(ml[2][0], ml[3][0]));
    float sc[4], l = 0.f;
#pragma unroll
    for (int i = 0; i < 4; ++i) { sc[i] = __builtin_amdgcn_exp2f(nmin - ml[i][0]); l += sc[i] * ml[i][1]; }
    const float inv = __builtin_amdgcn_rcpf(l);
    f32x4 o0 = {0, 0, 0, 0}, o1 = {0, 0, 0, 0};
#pragma unroll
    for (int i = 0; i < 4; ++i) {
      o0 += pr[2 * i] * (sc[i] * inv); o1 += pr[2 * i + 1] * (sc[i] * inv);
    }
    o8 = __builtin_bit_cast(u32x4, pack8(o0, o1));
    __builtin_amdgcn_raw_buffer_store_b128(o8, rg, go, 8 * NH * 4096 + w * 1024, 16);
  }
  const unsigned ol = lb + w * 4096 + lane * 16, o8l = lb + 16384 + (w & 1) * 1024 + lane * 16;
#pragma unroll
  for (int i = 0; i < 4; ++i) *(LDS_AS u32x4*)(uintptr_t)(ol + i * 1024) = of[i >> 1][i & 1];
  if (w < 2) *(LDS_AS u32x4*)(uintptr_t)o8l = o8;
  asm volatile("s_waitcnt lgkmcnt(0)\n\ts_barrier" ::: "memory");
  STAMPAT(0, 4);
#pragma unroll
  for (int i = 0; i < 4; ++i) *(LDS_AS u32x4*)(uintptr_t)(ol + OY + i * 1024) = of[i >> 1][2 + (i & 1)];
  if (w >= 2) *(LDS_AS u32x4*)(uintptr_t)(o8l + OY) = o8;

  if (!hh) {
    auto issue = [&](int t) {
      const unsigned u = ((hl + (t >> 2)) & 3) * 4 + (t & 1) * 2 + ((t >> 1) & 1), sb = lx + (t & 1) * HALF + (w & 1) * 9216;
      if (iw) {
#pragma unroll
        for (int i = 0; i < 9; ++i) dma16<0>(ro, vo[i], u * (64 * DM * 2), sb + WX + (t & 2) * (WX / 2) + i * 1024);
      } else {
#pragma unroll
        for (int i = 0; i < 9; ++i) dma16<2>(rg, vo[i], u * 2048, sb + (t & 2) * (OY / 2) + i * 1024);
      }
    };
    const int peer = (lane & 4) ? (blockIdx.x & ~3u) + (lane & 3) : (blockIdx.x & 63) + 64 * (lane & 3);
    unsigned* const mine = flags + blockIdx.x * 288 + (lane & 3) * 32;
    unsigned e = 0;
    if (iw) { issue(2); issue(3); }
    else e = __hip_atomic_load(flags + peer * 288 + 256, __ATOMIC_RELAXED, __HIP_MEMORY_SCOPE_AGENT) + TAG;
    const unsigned em = __builtin_amdgcn_readlane(e, hl);
    wait_bar<63>(); wait_bar<0>();
    if (!iw) {
      if (w == 0 && lane < 4) __hip_atomic_store(flags + peer * 288 + hl * 32, e, __ATOMIC_RELAXED, __HIP_MEMORY_SCOPE_AGENT);
      while (__any(__hip_atomic_load(mine, __ATOMIC_RELAXED, __HIP_MEMORY_SCOPE_AGENT) != em)) __builtin_amdgcn_s_sleep(1);
      asm volatile("" ::: "memory");
    }
    issue(4); issue(5);
#pragma clang loop unroll(disable)
    for (int t = 6; t < 16; ++t) { wait_bar<18>(); issue(t); }
    wait_bar<18>(); wait_bar<9>(); wait_bar<0>(); wait_bar<0>();
    asm volatile("s_barrier" ::: "memory");
    if (w == 0) {
      if ((lane >> 2) == 1) __hip_atomic_store(flags + peer * 288 + (4 + kq) * 32, e, __ATOMIC_RELAXED, __HIP_MEMORY_SCOPE_AGENT);
      while (__any(__hip_atomic_load(mine + 128, __ATOMIC_RELAXED, __HIP_MEMORY_SCOPE_AGENT) != em)) __builtin_amdgcn_s_sleep(1);
      asm volatile("s_barrier" ::: "memory");
      if (lane == 0) __hip_atomic_store(mine + 256, em, __ATOMIC_RELAXED, __HIP_MEMORY_SCOPE_AGENT);
    } else asm volatile("s_barrier" ::: "memory");
    return;
  }
  const rsrc_t rs = mk_rsrc(xch + (size_t)(blockIdx.x >> 2) * 176 * 1024, 176 * 1024);
  u32x2* po = out + (b * SQ * 2 * DM + br * DM + hl * 144) / 4;
  if (w == 0) oproj_body<0, 0, 5, 0, 4, true>(lx, lane, kq, rs, po STAMP_FWD);
  else if (w == 1) oproj_body<1, 0, 4, 4, 5, false>(lx, lane, kq, rs, po STAMP_FWD);
  else if (w == 2) oproj_body<2, 4, 5, 5, 4, false>(lx, lane, kq, rs, po STAMP_FWD);
  else oproj_body<3, 5, 4, 0, 5, false>(lx, lane, kq, rs, po STAMP_FWD);
  STAMPAT(1, 2);
}

extern "C" void kernel_launch(void* const* d_in, const int* in_sizes, int n_in, void* d_out, int out_size, void* d_ws, size_t ws_size, hipStream_t stream) {
  Ptrs pa = {d_in[0], d_in[1], d_in[2], d_in[3], d_in[4]}, pb = {d_in[5], d_in[6], d_in[7], d_in[8], d_in[9]};
  char* ws = (char*)d_ws;
  fused_kernel<<<256, 512, 0, stream>>>(pa, pb, ws, ws + (32u << 20), (unsigned*)(ws + (64u << 20)), (u32x2*)d_out STAMP_PASS);
}
```

```cpp
#include <hip/hip_runtime.h>
#include <hip/hip_bf16.h>
#include <stdint.h>

typedef __attribute__((ext_vector_type(8))) __bf16 bf16x8;
typedef __attribute__((ext_vector_type(4))) __bf16 bf16x4;
typedef __attribute__((ext_vector_type(2))) __bf16 bf16x2;
typedef __attribute__((ext_vector_type(4))) float f32x4;
typedef __attribute__((ext_vector_type(2))) float f32x2;
typedef __attribute__((ext_vector_type(4))) unsigned u32x4;
typedef __attribute__((ext_vector_type(2))) unsigned u32x2;
#define LDS_AS __attribute__((address_space(3)))
typedef __amdgpu_buffer_rsrc_t rsrc_t;

constexpr int NB = 8, SQ = 144, DM = 576, NH = 32, SK = 512, DH = 128, HD = NH * DH;


__device__ __forceinline__ rsrc_t mk_rsrc(const void* p, int bytes) {
  return __builtin_amdgcn_make_buffer_rsrc((void*)p, (short)0, bytes, 0x00020000);
}
template <int NT>
__device__ __forceinline__ void dma16(rsrc_t r, unsigned voff, unsigned soff, unsigned ldsb) {
  if constexpr (NT == 2)
    asm volatile("s_mov_b32 m0, %0\n\ts_nop 0\n\tbuffer_load_dwordx4 %1, %2, %3 offen sc1 lds" ::"s"(ldsb), "v"(voff), "s"(r), "s"(soff) : "memory");
  else if constexpr (NT)
    asm volatile("s_mov_b32 m0, %0\n\ts_nop 0\n\tbuffer_load_dwordx4 %1, %2, %3 offen nt lds" ::"s"(ldsb), "v"(voff), "s"(r), "s"(soff) : "memory");
  else
    asm volatile("s_mov_b32 m0, %0\n\ts_nop 0\n\tbuffer_load_dwordx4 %1, %2, %3 offen lds" ::"s"(ldsb), "v"(voff), "s"(r), "s"(soff) : "memory");
}
template <int NT>
__device__ __forceinline__ void dma4(rsrc_t r, unsigned voff, unsigned soff, unsigned ldsb) {
  if constexpr (NT)
    asm volatile("s_mov_b32 m0, %0\n\ts_nop 0\n\tbuffer_load_dword %1, %2, %3 offen nt lds" ::"s"(ldsb), "v"(voff), "s"(r), "s"(soff) : "memory");
  else
    asm volatile("s_mov_b32 m0, %0\n\ts_nop 0\n\tbuffer_load_dword %1, %2, %3 offen lds" ::"s"(ldsb), "v"(voff), "s"(r), "s"(soff) : "memory");
}
template <int NT>
__device__ __forceinline__ void dma16_if(rsrc_t r, unsigned voff, unsigned soff, unsigned ldsb, int a, int b) {
  if constexpr (NT)
    asm volatile("s_cmp_lg_u32 %4, %5\n\ts_cbranch_scc1 .Lsk%=\n\ts_mov_b32 m0, %0\n\ts_nop 0\n\tbuffer_load_dwordx4 %1, %2, %3 offen nt lds\n.Lsk%=:" ::"s"(ldsb), "v"(voff), "s"(r), "s"(soff), "s"(a), "s"(b) : "memory", "scc");
  else
    asm volatile("s_cmp_lg_u32 %4, %5\n\ts_cbranch_scc1 .Lsk%=\n\ts_mov_b32 m0, %0\n\ts_nop 0\n\tbuffer_load_dwordx4 %1, %2, %3 offen lds\n.Lsk%=:" ::"s"(ldsb), "v"(voff), "s"(r), "s"(soff), "s"(a), "s"(b) : "memory", "scc");
}
__device__ __forceinline__ void dma16nt_lt(rsrc_t r, unsigned voff, unsigned soff, unsigned ldsb, int add, int a, int b) {
  unsigned t;
  asm volatile("s_cmp_ge_u32 %5, %6\n\ts_cbranch_scc1 .Lsk%=\n\ts_add_u32 m0, %1, %7\n\ts_add_u32 %0, %4, %7\n\tbuffer_load_dwordx4 %2, %3, %0 offen nt lds\n.Lsk%=:" : "=&s"(t) : "s"(ldsb), "v"(voff), "s"(r), "s"(soff), "s"(a), "i"(b), "i"(add) : "memory", "scc");
}
template <int NT>
__device__ __forceinline__ void dma4_eq(rsrc_t r, unsigned voff, unsigned soff, unsigned ldsb, int a, int b) {
  if constexpr (NT)
    asm volatile("s_cmp_lg_u32 %3, %4\n\ts_cbranch_scc1 .Lsk%=\n\ts_mov_b32 m0, %0\n\ts_nop 0\n\tbuffer_load_dword %1, %2, %5 offen nt lds\n.Lsk%=:" ::"s"(ldsb), "v"(voff), "s"(r), "s"(a), "i"(b), "s"(soff) : "memory", "scc");
  else
    asm volatile("s_cmp_lg_u32 %3, %4\n\ts_cbranch_scc1 .Lsk%=\n\ts_mov_b32 m0, %0\n\ts_nop 0\n\tbuffer_load_dword %1, %2, %5 offen lds\n.Lsk%=:" ::"s"(ldsb), "v"(voff), "s"(r), "s"(a), "i"(b), "s"(soff) : "memory", "scc");
}
#define SB() __builtin_amdgcn_sched_barrier(0)
template <int N>
__device__ __forceinline__ void wait_bar() {
  asm volatile("s_waitcnt vmcnt(%0) lgkmcnt(0)\n\ts_barrier" ::"i"(N) : "memory");
}
template <int N0, int N>
__device__ __forceinline__ void wait_bar2(int a, int b) {
  asm volatile("s_cmp_eq_u32 %2, %3\n\ts_cbranch_scc1 .Lw2%=\n\ts_waitcnt vmcnt(%1)\n.Lw2%=:\n\ts_waitcnt vmcnt(%0) lgkmcnt(0)\n\ts_barrier" ::"i"(N0), "i"(N), "s"(a), "s"(b) : "memory", "scc");
}
__device__ __forceinline__ bf16x8 lds_b128(unsigned a) { return *(const LDS_AS bf16x8*)(uintptr_t)a; }
__device__ __forceinline__ bf16x4 lds_tr(unsigned a) { return __builtin_amdgcn_ds_read_tr16_b64_v4bf16((LDS_AS bf16x4*)(uintptr_t)a); }
__device__ __forceinline__ bf16x8 lds_tr2(unsigned a0, unsigned a1) {
  bf16x4 x = lds_tr(a0), y = lds_tr(a1);
  return __builtin_shufflevector(x, y, 0, 1, 2, 3, 4, 5, 6, 7);
}
__device__ __forceinline__ f32x4 mfma(bf16x8 a, bf16x8 b, f32x4 c) { return __builtin_amdgcn_mfma_f32_16x16x32_bf16(a, b, c, 0, 0, 0); }
__device__ __forceinline__ void mfma_p(bf16x8 a, bf16x8 b, f32x4& c) { c = mfma(a, b, c); asm volatile("" : "+v"(c)); }
__device__ __forceinline__ unsigned pk2(float a, float b) {
  f32x2 v = {a, b};
  bf16x2 r = __builtin_convertvector(v, bf16x2);
  return __builtin_bit_cast(unsigned, r);
}
__device__ __forceinline__ bf16x8 pack8(f32x4 a, f32x4 b) {
  u32x4 w = {pk2(a[0], a[1]), pk2(a[2], a[3]), pk2(b[0], b[1]), pk2(b[2], b[3])};
  return __builtin_bit_cast(bf16x8, w);
}
__device__ __forceinline__ float fmx(float a, float b) { return __builtin_elementwise_maximum(a, b); }
__device__ __forceinline__ float gmax(float v) {
  auto a = __builtin_amdgcn_permlane16_swap(__float_as_uint(v), __float_as_uint(v), false, false);
  v = fmx(__uint_as_float(a[0]), __uint_as_float(a[1]));
  auto b = __builtin_amdgcn_permlane32_swap(__float_as_uint(v), __float_as_uint(v), false, false);
  return fmx(__uint_as_float(b[0]), __uint_as_float(b[1]));
}
__device__ __forceinline__ float gsum(float v) {
  auto a = __builtin_amdgcn_permlane16_swap(__float_as_uint(v), __float_as_uint(v), false, false);
  v = __uint_as_float(a[0]) + __uint_as_float(a[1]);
  auto b = __builtin_amdgcn_permlane32_swap(__float_as_uint(v), __float_as_uint(v), false, false);
  return __uint_as_float(b[0]) + __uint_as_float(b[1]);
}
__device__ __forceinline__ float max4(f32x4 a) { return fmx(fmx(a[0], a[1]), fmx(a[2], a[3])); }

#define NT_KV 1
#define PP 1
#define BURST 0
#define LIM 6
#define CODE 49152
#define KS 0
#define VS0 1
#define PRI 1
#define NTI 1
#define NTO 0
#define KPX (((b + hl) & 1) * 4)
#define GA 0
#define GD 3
#define NV 1
#define WG0 6
#define NT_W 0
constexpr float QSCALE = 0.08838834764831845f * 1.4426950408889634f;
constexpr float PLIM = 4096.f;
constexpr float PMIN = 1.f / 4096.f;
constexpr int SLOT = 16384, HALF = 81920, XB0 = 49152, XB1 = HALF + XB0, Q8OFF = 77824;
__device__ __forceinline__ constexpr int wbuf(int i) { return i == 0 ? 32768 : i == 1 ? 40960 : 67584; }

struct Ptrs { const void *x, *k, *v, *wq, *wo; };

constexpr int WX = 18432, OY = 3 * WX;
constexpr unsigned TAG = 0x9E3779B9u;
template <int W, int Q0, int NQ, int N0, int NN, bool CEN>
__device__ __forceinline__ void oproj_body(unsigned lb, int lane, int kq, rsrc_t rx, u32x2* __restrict__ out) {
  const int c16 = lane & 15, g = lane >> 4;
  constexpr int NW = NN + CEN, NM = NQ * NN + CEN;
  f32x4 acc[NM];
#pragma unroll
  for (int m = 0; m < NM; ++m) acc[m] = f32x4{0, 0, 0, 0};
  const unsigned ao0 = lb + Q0 * 2048 + lane * 16;
  const unsigned aw0 = lb + 18432 + (4 * g + (c16 >> 2)) * 288 + N0 * 32 + 8 * (c16 & 3);
  unsigned ao = ao0, aw = aw0;
  bf16x8 of[2][NQ]; bf16x4 wf[2][NW][2];
  auto rd = [&](int ks, int i) {
    const int f = i / 3, r = i % 3;
    if (r == 2) { if (f < NQ) of[ks][f] = lds_b128(ao + f * 2048 + ks * 1024); }
    else if (f < NW) wf[ks][f][r] = lds_tr(aw + f * 32 + (ks * 32 + 16 * r) * 288);
  };
  auto half = [&](int ks) {
#pragma unroll
    for (int m = 0; m < NM; ++m) {
      SB();
      const int t = m < NQ * NN ? m / NQ : NW - 1, a = m < NQ * NN ? m % NQ : NQ - 1;
      acc[m] = mfma(__builtin_shufflevector(wf[ks][t][0], wf[ks][t][1], 0, 1, 2, 3, 4, 5, 6, 7), of[ks][a], acc[m]);
      SB();
      if (m < 15) rd(ks ^ 1, m);
    }
    SB();
  };
#pragma unroll
  for (int i = 0; i < 15; ++i) rd(0, i);
#pragma clang loop unroll(disable)
  for (int t = 1; t <= 16; ++t) {
    half(0);
    ao = ao0 + ((t & 1) * HALF + (t & 2) * (OY / 2)); aw = aw0 + ((t & 1) * HALF + (t & 2) * (WX / 2));
    asm volatile("s_cmp_lg_u32 %2, 2\n\ts_cbranch_scc1 .Lo%=\n\ts_waitcnt vmcnt(0)\n.Lo%=:\n\ts_waitcnt lgkmcnt(0)\n\ts_barrier" : "+v"(ao), "+v"(aw) : "s"(t) : "memory", "scc");
    half(1);
  }
  constexpr int NP = (NM + 1) / 2;
  auto fin = [&](auto kc) {
    constexpr int K = decltype(kc)::value;
#pragma unroll
    for (int p = 0; p < NP; ++p)
      if (((p + 2 * W) & 3) != K) __builtin_amdgcn_raw_buffer_store_b128(__builtin_bit_cast(u32x4, pack8(acc[2 * p], acc[2 * p + 1 < NM ? 2 * p + 1 : 2 * p])), rx, lane * 16, ((K * 4 + W) * 11 + p) * 1024, 16);
    asm volatile("s_waitcnt vmcnt(0)\n\ts_barrier" ::: "memory");
    asm volatile("s_barrier" ::: "memory");
    u32x4 in[3][3];
#pragma unroll
    for (int p = 0; p < NP; ++p)
      if (((p + 2 * W) & 3) == K) {
#pragma unroll
        for (int i = 0; i < 3; ++i) in[p >> 2][i] = __builtin_amdgcn_raw_buffer_load_b128(rx, lane * 16, ((((K + 1 + i) & 3) * 4 + W) * 11 + p) * 1024, 16);
      }
#pragma unroll
    for (int m = 0; m < NM; ++m)
      if ((((m >> 1) + 2 * W) & 3) == K) {
        f32x4 v = acc[m];
#pragma unroll
        for (int i = 0; i < 3; ++i) {
          const unsigned u0 = in[m >> 3][i][2 * (m & 1)], u1 = in[m >> 3][i][2 * (m & 1) + 1];
          v += f32x4{__uint_as_float(u0 << 16), __uint_as_float(u0 & 0xffff0000u), __uint_as_float(u1 << 16), __uint_as_float(u1 & 0xffff0000u)};
        }
        const int qt = m < NQ * NN ? Q0 + m % NQ : 4, nt = m < NQ * NN ? N0 + m / NQ : 4;
        out[(qt * 16 + c16) * (2 * DM / 4) + nt * 4 + g] = u32x2{pk2(v[0], v[1]), pk2(v[2], v[3])};
      }
  };
  using std::integral_constant;
  if (kq == 0) fin(integral_constant<int, 0>{}); else if (kq == 1) fin(integral_constant<int, 1>{});
  else if (kq == 2) fin(integral_constant<int, 2>{}); else fin(integral_constant<int, 3>{});
}
__global__ __launch_bounds__(512) void fused_kernel(Ptrs pa, Ptrs pb, char* ows, char* xch, unsigned* flags, u32x2* __restrict__ out) {
  __shared__ __attribute__((aligned(16))) char smem[2 * HALF];
  const int lane = threadIdx.x & 63, w8 = __builtin_amdgcn_readfirstlane(threadIdx.x >> 6), w = w8 & 3, hh = w8 >> 2;
  const unsigned lx = (unsigned)(uintptr_t)(LDS_AS char*)smem, lb = lx + hh * HALF;
  const int c16 = lane & 15, g = lane >> 4;
  const int xcd = blockIdx.x & 7, j = blockIdx.x >> 3;
  const int br = xcd >> 2, b = j & 7, hl = j >> 3, h = (xcd & 3) * 8 + 2 * hl + hh;
  const Ptrs p = br ? pb : pa;
  const rsrc_t rx = mk_rsrc((const char*)p.x + (size_t)b * SQ * DM * 2, SQ * DM * 2);
  const rsrc_t rw = mk_rsrc((const char*)p.wq + h * DH * 2, DM * HD * 2 - h * DH * 2);
  const rsrc_t rk = mk_rsrc((const char*)p.k + (size_t)(b * NH + h) * SK * DH * 2, SK * DH * 2);
  const rsrc_t rv = mk_rsrc((const char*)p.v + (size_t)(b * NH + h) * SK * DH * 2, SK * DH * 2);

  const unsigned fw = (g & 1) | ((g & 2) << 1) | ((w & 1) << 3);
  const unsigned vo_w = (8 * w + g) * (HD * 2) + ((c16 ^ fw) << 4);
  const unsigned vo_x0 = (lane >> 3) * (DM * 2) + (((lane & 7) ^ g) << 4), vo_x1 = (vo_x0 ^ 64) + 8 * DM * 2;
  const int r8 = (lane >> 2) & 7;
  const unsigned vo_kv = (8 * (w >> 1) + r8) * 256 + (2 * (w & 1) + (lane >> 5)) * 64;
  const unsigned vo_k = vo_kv + (((lane & 3) ^ (w & 2)) << 4);
  const unsigned vo_v = vo_kv + (((lane & 3) ^ ((r8 & 4) >> 1)) << 4);

  auto iss_w = [&](int kc, int buf, int i) { dma16<NT_W>(rw, vo_w, (kc * 32 + 4 * i) * HD * 2, lb + wbuf(buf) + w * 2048 + i * 1024); };
  auto iss_x = [&](int kp, unsigned xb, int i) { dma16<NT_W>(rx, i ? vo_x1 : vo_x0, kp * 128 + w8 * 16 * DM * 2, xb + w8 * 2048 + i * 1024); };
  auto iss_x8 = [&](int kp, unsigned xb, int i, int who) { dma16_if<NT_W>(rx, i ? vo_x1 : vo_x0, kp * 128 + 128 * DM * 2, xb + 16384 + i * 1024, w8, who); };
  auto iss_kv = [&](int t, int i) {
    const unsigned sb = lb + (t % 5) * SLOT + w * 1024, so = (t >> 1) * SLOT;
    if (t & 1) dma16<NT_KV>(rv, vo_v, so + i * 4096, sb + i * 4096);
    else dma16<NT_KV>(rk, vo_k, so + i * 4096, sb + i * 4096);
  };

  int kp = KPX; kp = kp >= 9 ? kp - 9 : kp;
  int kc = 2 * kp;
  auto nkc = [&]() { kc = kc == 17 ? 0 : kc + 1; };
  auto nkp = [&]() { kp = kp == 8 ? 0 : kp + 1; };
  auto ini_w = [&](int kc, int buf, int i) { dma16<NTI & 1>(rw, vo_w, (kc * 32 + 4 * i) * HD * 2, lb + wbuf(buf) + w * 2048 + i * 1024); };
  auto ini_x = [&](int kp, unsigned xb, int i) { dma16<(NTI >> 1)>(rx, i ? vo_x1 : vo_x0, kp * 128 + w8 * 16 * DM * 2, xb + w8 * 2048 + i * 1024); };
  iss_x8(kp, lx + XB0, 0, 0); iss_x8(kp, lx + XB0, 1, 1);
  ini_x(kp, lx + XB0, 0); ini_x(kp, lx + XB0, 1); nkp();
  ini_w(kc, 0, 0); ini_w(kc, 0, 1); nkc();
  asm volatile("s_barrier" ::: "memory");
  iss_x8(kp, lx + XB1, 0, 2); iss_x8(kp, lx + XB1, 1, 3);
  ini_w(kc, 1, 0); ini_w(kc, 1, 1); nkc(); ini_x(kp, lx + XB1, 0); ini_x(kp, lx + XB1, 1); nkp();
  ini_w(kc, 2, 0); ini_w(kc, 2, 1); nkc();
  {
    unsigned long long pc; asm volatile("s_getpc_b64 %0" : "=s"(pc));
    dma4<0>(mk_rsrc((const char*)(pc & ~127ull), w8 == 7 ? CODE : 0), (j & 7) * 8192 + lane * 128, 0, lb + 75776 + w * 256);
  }
  f32x4 qa[2][8], q8[2];
#pragma unroll
  for (int t = 0; t < 8; ++t) { qa[0][t] = f32x4{0, 0, 0, 0}; qa[1][t] = f32x4{0, 0, 0, 0}; }
  q8[0] = f32x4{0, 0, 0, 0}; q8[1] = f32x4{0, 0, 0, 0};
  unsigned aw[8];
  {
    const int qq = c16 >> 2, pp = c16 & 3;
    const unsigned f = (qq & 1) | ((qq & 2) << 1) | ((g & 1) << 3);
#pragma unroll
    for (int s = 0; s < 4; ++s) { aw[2 * s] = lb + wbuf(0) + (8 * g + qq) * 256 + ((((unsigned)(4 * (s ^ w) + pp)) ^ f) << 4) + 8 * (pp & 1); aw[2 * s + 1] = aw[2 * s] ^ 8; }
  }
  const unsigned axl = c16 * 128 + ((g ^ (c16 >> 1)) << 4);
  unsigned xrd = lx + XB0, xwr = lx + XB0;
  bf16x8 fx[2][3], fy[3]; bf16x4 fq[2][8][2];
  unsigned ax[4];
  auto x_addr = [&]() { ax[0] = axl + (xrd + w * 4096); ax[1] = ax[0] ^ 64; ax[2] = axl + (xrd + 16384); ax[3] = ax[2] ^ 64; xrd = 2 * lx + XB0 + XB1 - xrd; };
  auto rd_w = [&](int set, int buf, int i) { fq[set][i >> 1][i & 1] = lds_tr(aw[i >> 1] + (wbuf(buf) - wbuf(0)) + (i & 1) * 1024); };
  auto rd_x = [&](int i) {
    const bf16x8 v = lds_b128(i % 3 == 2 ? ax[2 + i / 3] : ax[i / 3] + (i % 3) * 2048);
    if (i < 3) fx[0][i] = v; else fy[i - 3] = v;
  };
  x_addr();
  wait_bar<7>();
#pragma unroll
  for (int i = 0; i < 16; ++i) rd_w(0, 0, i);
#pragma unroll
  for (int i = 0; i < 6; ++i) rd_x(i);
  rsrc_t rt = rk; unsigned vt = vo_k, lt = lb + w * 1024;
  const rsrc_t rp = mk_rsrc((const char*)p.wo + ((size_t)h * DH + b * 16) * DM * 2 + w * 4608, 4608);
  asm volatile("s_waitcnt vmcnt(5)" ::: "memory");
  auto q_step = [&](auto uc, auto tailc, int who, int prev) {
    constexpr int u = decltype(uc)::value, cs = u & 1; constexpr bool TAIL = decltype(tailc)::value;
    if (cs) {
#pragma unroll
      for (int i = 0; i < 3; ++i) fx[1][i] = fy[i];
      x_addr();
    }
    constexpr int NF = u == 0 || u == 4 ? 6 : u == 5 ? 5 : 4;
    if (!TAIL || u < 3) wait_bar2<NF + 1, NF>(w8, prev);
    else if (u < 5) wait_bar<u == 3 ? 2 : 0>();
    int gap = 0;
    auto fill = [&]() {
      SB();
      if (!(TAIL && u == 5)) {
        constexpr int G0 = WG0;
        if (gap >= G0 && gap < G0 + 8) { rd_w(cs ^ 1, (u + 1) % 3, 2 * (gap - G0)); rd_w(cs ^ 1, (u + 1) % 3, 2 * (gap - G0) + 1); }
        else if (cs && gap < 14) rd_x(gap < G0 ? gap : gap - 8);
      }
      if ((!TAIL || u < 3) && (gap == GA || gap == GA + GD)) iss_w(kc, u % 3, gap != GA);
      if ((!TAIL || u < 2) && gap == GA + 2 * GD) iss_x8(kp, xwr, cs, who);
      if ((!TAIL || u < 2) && gap == GA + 3 * GD) iss_x(kp, xwr, cs);
      if (!TAIL && u >= 2 && gap == GA + 4 * GD) dma16<NT_KV>(rt, vt, (u - 2) * 4096, lt + (u - 2) * 4096);
      if (TAIL && u > 3 && gap >= 2 && gap < 14 && gap % 3 == 2) iss_kv(u - 2, (gap - 2) / 3);
      ++gap;
      SB();
    };
#pragma unroll
    for (int f = 0; f < 8; ++f) {
      const bf16x8 wf = __builtin_shufflevector(fq[cs][f][0], fq[cs][f][1], 0, 1, 2, 3, 4, 5, 6, 7);
      mfma_p(wf, fx[cs][0], qa[0][f]); fill();
      mfma_p(wf, fx[cs][1], qa[1][f]); fill();
      if (f < 2) { mfma_p(wf, fx[cs][2], q8[f]); fill(); }
    }
    nkc();
    if (cs) { nkp(); xwr = 2 * lx + XB0 + XB1 - xwr; }
  };
  using std::integral_constant;
#define QS(u, tl, who, prev) q_step(integral_constant<int, u>{}, integral_constant<bool, tl>{}, who, prev)
#pragma clang loop unroll(disable)
  for (int it = 0; it < 8; it += 4) { QS(0, false, it, 15 - 2 * it); QS(1, false, it + 1, it); QS(2, false, it + 2, it + 1); QS(3, false, it + 3, it + 2); QS(4, false, it ^ 6, it + 3); QS(5, false, it ^ 7, it ^ 6); rt = rv; vt = vo_v; lt += SLOT; }
  QS(0, true, 0, 3); QS(1, true, 1, 0); QS(2, true, 0, 1); QS(3, true, 0, 0); QS(4, true, 0, 0); QS(5, true, 0, 0);
  bf16x8 qf[3][4];
  auto qpack = [&](f32x4 lo, f32x4 hi) {
    lo *= QSCALE; hi *= QSCALE;
    const unsigned a0 = pk2(lo[0], lo[1]), a1 = pk2(lo[2], lo[3]), b0 = pk2(hi[0], hi[1]), b1 = pk2(hi[2], hi[3]);
    const bool odd = g & 1;
    u32x4 r = {odd ? b0 : a0, odd ? b1 : a1, odd ? a0 : b0, odd ? a1 : b1};
    return __builtin_bit_cast(bf16x8, r);
  };
  *(LDS_AS bf16x8*)(uintptr_t)(lb + Q8OFF + w * 1024 + lane * 16) = qpack(q8[0], q8[1]);
  wait_bar<8>();
#pragma unroll
  for (int s = 0; s < 4; ++s) qf[2][s] = lds_b128(lb + Q8OFF + s * 1024 + lane * 16);
  if (PP && hh) asm volatile("s_barrier" ::: "memory");
#pragma unroll
  for (int t = 0; t < 8; ++t) asm volatile("" : "+v"(qa[0][t]), "+v"(qa[1][t]));
#pragma unroll
  for (int s = 0; s < 4; ++s) { qf[0][s] = qpack(qa[0][2 * s], qa[0][2 * s + 1]); qf[1][s] = qpack(qa[1][2 * s], qa[1][2 * s + 1]); }
#pragma unroll
  for (int a = 0; a < 2; ++a) {
    if (w & 1) { bf16x8 t = qf[a][0]; qf[a][0] = qf[a][1]; qf[a][1] = t; t = qf[a][2]; qf[a][2] = qf[a][3]; qf[a][3] = t; }
    if (w & 2) { bf16x8 t = qf[a][0]; qf[a][0] = qf[a][2]; qf[a][2] = t; t = qf[a][1]; qf[a][1] = qf[a][3]; qf[a][3] = t; }
  }
  f32x4 oa[3][8];
#pragma unroll
  for (int t = 0; t < 8; ++t) { oa[0][t] = f32x4{0, 0, 0, 0}; oa[1][t] = f32x4{0, 0, 0, 0}; oa[2][t] = f32x4{0, 0, 0, 0}; }
  float ls[3] = {0.f, 0.f, 0.f};
  f32x4 nv[3] = {{0, 0, 0, 0}, {0, 0, 0, 0}, {0, 0, 0, 0}};
  const unsigned ak = lb + (c16 >> 3) * 2048 + (c16 & 7) * 64 + ((g ^ ((c16 & 8) >> 2)) << 4);
  const unsigned av = lb + (g >> 1) * 2048 + (4 * (g & 1) + (c16 >> 2)) * 64 + (g & 1) * 32 + 8 * (c16 & 3);
  const int kq = xcd & 3;
  const rsrc_t ro = mk_rsrc((const char*)p.wo + (size_t)kq * 1024 * DM * 2 + hl * 288, 1024 * DM * 2 - hl * 288);
  const unsigned vo_a = ((w8 * 64 + lane) / 18) * (DM * 2) + ((w8 * 64 + lane) % 18) * 16, vo_b = ((512 + lane) / 18) * (DM * 2) + ((512 + lane) % 18) * 16;
  unsigned ko = 0;
#pragma clang loop unroll(disable)
  for (int n0 = 0; n0 < 8; ++n0) {
    int n = n0; asm volatile("" : "+s"(n));
    const unsigned vo = ko == 4 * SLOT ? 0 : ko + SLOT, po = ko == 0 ? 4 * SLOT : ko - SLOT;
    unsigned kb[4];
#pragma unroll
    for (int kt = 0; kt < 4; ++kt) kb[kt] = ak + (ko + (kt ^ w) * 4096);
    asm volatile("s_cmp_lt_u32 %0, 7\n\ts_cbranch_scc1 .Lwa%=\n\ts_waitcnt vmcnt(5)\n.Lwa%=:\n\ts_waitcnt vmcnt(12) lgkmcnt(0)\n\ts_barrier" ::"s"(n) : "memory", "scc");
    if (PRI) __builtin_amdgcn_s_setprio(PRI & 1);
    bf16x8 kf[2][4];
#pragma unroll
    for (int s = 0; s < 4; ++s) kf[0][s] = lds_b128(kb[0] + s * 512);
    SB();
    unsigned gso = (n + 2) * SLOT, kld = lb + po + w * 1024, vld = lb + ko + w * 1024;
    asm volatile("" : "+s"(gso), "+s"(kld), "+s"(vld));
    auto iss_k = [&](int i, int lim) { dma16nt_lt(rk, vo_k, gso, kld, i * 4096, n, lim); };
    f32x4 sa[2][4], s8;
    float ts[3];
    bf16x8 pf[2][2], p8;
    auto ex = [&](f32x4& v, float& t, bool init) {
#pragma unroll
      for (int r = 0; r < 4; ++r) { v[r] = __builtin_amdgcn_exp2f(v[r]); if (init && r == 0) t = v[0]; else t += v[r]; }
    };
    auto packs = [&]() {
      pf[0][0] = pack8(sa[0][0], sa[0][1]); pf[1][0] = pack8(sa[1][0], sa[1][1]); pf[0][1] = pack8(sa[0][2], sa[0][3]); pf[1][1] = pack8(sa[1][2], sa[1][3]);
      u32x4 t = {pk2(s8[0], s8[1]), pk2(s8[2], s8[3]), 0u, 0u}; p8 = __builtin_bit_cast(bf16x8, t);
    };
    bool ok;
    {
#pragma unroll
      for (int kt = 0; kt < 4; ++kt) {
        sa[0][kt] = nv[0]; sa[1][kt] = nv[1];
        if (kt == 0) s8 = nv[2];
#pragma unroll
        for (int s = 0; s < 4; ++s) {
          SB();
          sa[0][kt] = mfma(kf[kt & 1][s], qf[0][s], sa[0][kt]);
          if (kt < 3) {
 kf[(kt & 1) ^ 1][s] = lds_b128(kb[kt + 1] + s * 512);
}
          sa[1][kt] = mfma(kf[kt & 1][s], qf[1][s], sa[1][kt]);
          if (kt == 0) s8 = mfma(kf[0][s], qf[2][s], s8);
          if (BURST ? kt == KS : s == KS) { SB(); iss_k(BURST ? s : kt, LIM); if ((BURST ? s : kt) == 0) dma4_eq<NTO>(rp, lane * 128, 0, lb + SLOT + w * 256, n, 6); }
          if (kt > 0 && s == 1) ex(sa[0][kt - 1], ts[0], kt == 1);
          if (kt > 0 && s == 2) ex(sa[1][kt - 1], ts[1], kt == 1);
          if (kt == 1 && s == 3) ex(s8, ts[2], true);
        }
      }
      SB();
      ex(sa[0][3], ts[0], false); ex(sa[1][3], ts[1], false);
      packs();
      ok = !__any(!(ts[0] + ts[1] + ts[2] <= PLIM) || !(fminf(fminf(ts[0], ts[1]), ts[2]) >= (n ? 0.f : PMIN)));
    }
    if (__builtin_expect(!ok, 0)) {
      asm volatile("" : "+v"(kb[0]), "+v"(kb[1]), "+v"(kb[2]), "+v"(kb[3]));
#pragma unroll
      for (int kt = 0; kt < 4; ++kt) {
        sa[0][kt] = nv[0]; sa[1][kt] = nv[1];
        if (kt == 0) s8 = nv[2];
#pragma unroll
        for (int s = 0; s < 4; ++s) kf[0][s] = lds_b128(kb[kt] + s * 512);
#pragma unroll
        for (int s = 0; s < 4; ++s) {
          sa[0][kt] = mfma(kf[0][s], qf[0][s], sa[0][kt]);
          sa[1][kt] = mfma(kf[0][s], qf[1][s], sa[1][kt]);
          if (kt == 0) s8 = mfma(kf[0][s], qf[2][s], s8);
        }
      }
      float d0 = gmax(fmx(fmx(max4(sa[0][0]), max4(sa[0][1])), fmx(max4(sa[0][2]), max4(sa[0][3]))));
      float d1 = gmax(fmx(fmx(max4(sa[1][0]), max4(sa[1][1])), fmx(max4(sa[1][2]), max4(sa[1][3]))));
      float d2 = gmax(max4(s8));
      if (n != 0) {
        d0 = fmx(d0, 0.f); d1 = fmx(d1, 0.f); d2 = fmx(d2, 0.f);
        const float a0 = __builtin_amdgcn_exp2f(-d0), a1 = __builtin_amdgcn_exp2f(-d1), a2 = __builtin_amdgcn_exp2f(-d2);
        ls[0] *= a0; ls[1] *= a1; ls[2] *= a2;
#pragma unroll
        for (int t = 0; t < 8; ++t) { oa[0][t] *= a0; oa[1][t] *= a1; oa[2][t] *= a2; }
      }
      nv[0] -= d0; nv[1] -= d1; nv[2] -= d2;
      if (NV) asm volatile("" : "+v"(nv[0]), "+v"(nv[1]), "+v"(nv[2]));
#pragma unroll
      for (int kt = 0; kt < 4; ++kt) { sa[0][kt] -= d0; sa[1][kt] -= d1; ex(sa[0][kt], ts[0], kt == 0); ex(sa[1][kt], ts[1], kt == 0); }
      s8 -= d2; ex(s8, ts[2], true);
      packs();
    }
    ls[0] += ts[0]; ls[1] += ts[1]; ls[2] += ts[2];
    unsigned vb[4];
#pragma unroll
    for (int i = 0; i < 4; ++i) vb[i] = av + (vo + (i ^ w) * 4096);
    asm volatile("s_cmp_lt_u32 %0, 6\n\ts_cbranch_scc1 .Lwb%=\n\ts_waitcnt vmcnt(9)\n\ts_cmp_lt_u32 %0, 7\n\ts_cbranch_scc1 .Lwb%=\n\ts_waitcnt vmcnt(1)\n.Lwb%=:\n\ts_waitcnt vmcnt(12) lgkmcnt(0)\n\ts_barrier" ::"s"(n) : "memory", "scc");
    if (PRI) __builtin_amdgcn_s_setprio(PRI >> 1);
    bf16x8 vf[4];
    auto rd_v = [&](int i) { const int m = i >> 3, t = (i & 3) * 2 + ((i >> 2) & 1); vf[i & 3] = lds_tr2((vb[2 * m] ^ ((t & 1) * 32)) + (t >> 1) * 512, (vb[2 * m + 1] ^ ((t & 1) * 32)) + (t >> 1) * 512); };
    rd_v(0); rd_v(1); rd_v(2);
    SB();
#pragma unroll
    for (int i = 0; i < 16; ++i) {
      const int m = i >> 3, t = (i & 3) * 2 + ((i >> 2) & 1);
      SB();
      oa[0][t] = mfma(vf[i & 3], pf[0][m], oa[0][t]);
      if (i < 13) rd_v(i + 3);
      oa[1][t] = mfma(vf[i & 3], pf[1][m], oa[1][t]);
      if (m == 0) oa[2][t] = mfma(vf[i & 3], p8, oa[2][t]);
      if (BURST ? (i >= VS0 && i < VS0 + 4) : (i == VS0 || i == VS0 + 3 || i == VS0 + 6 || i == VS0 + 10)) { SB(); const int q = (i - VS0) / (BURST ? 1 : 3); dma16nt_lt(rv, vo_v, gso, vld, q * 4096, n, LIM);
        dma16_if<0>(ro, vo_a, (hl * 256 + (q >> 1) * 128 + (q & 1) * 32) * DM * 2, lx + (q >> 1) * HALF + WX + ((q & 1) * 9 + w8) * 1024, n, 7);
        if (q == 3) dma16_if<0>(ro, vo_b, (hl * 256 + (w >> 1) * 128 + (w & 1) * 32) * DM * 2, lx + (w >> 1) * HALF + WX + ((w & 1) * 9 + 8) * 1024, n + 8 * hh, 7);
      }
    }
    SB();
    ko = vo == 4 * SLOT ? 0 : vo + SLOT;
  }
  if (PP && !hh) asm volatile("s_barrier" ::: "memory");
  const rsrc_t rg = mk_rsrc(ows + ((size_t)(br * NB + b) * 9 * NH + kq * 8) * 4096, (9 * NH - kq * 8) * 4096);
  const unsigned go = (2 * hl + hh) * 4096 + lane * 16;
  const unsigned mb = lb + 47104;
  const float l8 = gsum(ls[2]);
#pragma unroll
  for (int t = 0; t < 8; ++t) *(LDS_AS f32x4*)(uintptr_t)(mb + w * 8192 + t * 1024 + lane * 16) = oa[2][t];
  *(LDS_AS f32x2*)(uintptr_t)(mb + 32768 + w * 512 + lane * 8) = f32x2{nv[2][0], l8};
  u32x4 of[2][4], o8;
#pragma unroll
  for (int a = 0; a < 2; ++a) {
    const float inv = __builtin_amdgcn_rcpf(gsum(ls[a]));
#pragma unroll
    for (int s = 0; s < 4; ++s) {
      of[a][s] = __builtin_bit_cast(u32x4, pack8(oa[a][2 * s] * inv, oa[a][2 * s + 1] * inv));
      __builtin_amdgcn_raw_buffer_store_b128(of[a][s], rg, go + s * 1024, (2 * w + a) * NH * 4096, 16);
    }
  }
  const bool iw = w >= 2;
  unsigned vo[9];
  if (!hh) {
    unsigned r = lane % 18;
    vo[0] = iw ? (lane / 18 + (w & 1) * 32) * (DM * 2) + r * 16 : (w & 1) * (4 * NH * 4096 + 1024) + lane * 16;
#pragma unroll
    for (int i = 1; i < 9; ++i) {
      r += 10;
      const bool c = r >= 18;
      r = c ? r - 18 : r;
      const unsigned so = ((w + i) & 1) ? 1024 : NH * 4096 - 1024, d0 = iw ? 3616 : so, d1 = iw ? 4480 : so;
      vo[i] = vo[i - 1] + (c ? d1 : d0);
    }
#pragma unroll
    for (int i = 0; i < 9; ++i) asm volatile("" : "+v"(vo[i]));
  }
  wait_bar<8>();
  {
    f32x2 ml[4]; f32x4 pr[8];
#pragma unroll
    for (int i = 0; i < 4; ++i) ml[i] = *(const LDS_AS f32x2*)(uintptr_t)(mb + 32768 + i * 512 + lane * 8);
#pragma unroll
    for (int i = 0; i < 8; ++i) pr[i] = *(const LDS_AS f32x4*)(uintptr_t)(mb + w * 2048 + lane * 16 + (i >> 1) * 8192 + (i & 1) * 1024);
    SB();
    const float nmin = fminf(fminf(ml[0][0], ml[1][0]), fminf(ml[2][0], ml[3][0]));
    float sc[4], l = 0.f;
#pragma unroll
    for (int i = 0; i < 4; ++i) { sc[i] = __builtin_amdgcn_exp2f(nmin - ml[i][0]); l += sc[i] * ml[i][1]; }
    const float inv = __builtin_amdgcn_rcpf(l);
    f32x4 o0 = {0, 0, 0, 0}, o1 = {0, 0, 0, 0};
#pragma unroll
    for (int i = 0; i < 4; ++i) {
      o0 += pr[2 * i] * (sc[i] * inv); o1 += pr[2 * i + 1] * (sc[i] * inv);
    }
    o8 = __builtin_bit_cast(u32x4, pack8(o0, o1));
    __builtin_amdgcn_raw_buffer_store_b128(o8, rg, go, 8 * NH * 4096 + w * 1024, 16);
  }
  const unsigned ol = lb + w * 4096 + lane * 16, o8l = lb + 16384 + (w & 1) * 1024 + lane * 16;
#pragma unroll
  for (int i = 0; i < 4; ++i) *(LDS_AS u32x4*)(uintptr_t)(ol + i * 1024) = of[i >> 1][i & 1];
  if (w < 2) *(LDS_AS u32x4*)(uintptr_t)o8l = o8;
  asm volatile("s_waitcnt lgkmcnt(0)\n\ts_barrier" ::: "memory");
#pragma unroll
  for (int i = 0; i < 4; ++i) *(LDS_AS u32x4*)(uintptr_t)(ol + OY + i * 1024) = of[i >> 1][2 + (i & 1)];
  if (w >= 2) *(LDS_AS u32x4*)(uintptr_t)(o8l + OY) = o8;

  if (!hh) {
    auto issue = [&](int t) {
      const unsigned u = ((hl + (t >> 2)) & 3) * 4 + (t & 1) * 2 + ((t >> 1) & 1), sb = lx + (t & 1) * HALF + (w & 1) * 9216;
      if (iw) {
#pragma unroll
        for (int i = 0; i < 9; ++i) dma16<0>(ro, vo[i], u * (64 * DM * 2), sb + WX + (t & 2) * (WX / 2) + i * 1024);
      } else {
#pragma unroll
        for (int i = 0; i < 9; ++i) dma16<2>(rg, vo[i], u * 2048, sb + (t & 2) * (OY / 2) + i * 1024);
      }
    };
    const int peer = (lane & 4) ? (blockIdx.x & ~3u) + (lane & 3) : (blockIdx.x & 63) + 64 * (lane & 3);
    unsigned* const mine = flags + blockIdx.x * 288 + (lane & 3) * 32;
    unsigned e = 0;
    if (iw) { issue(2); issue(3); }
    else e = __hip_atomic_load(flags + peer * 288 + 256, __ATOMIC_RELAXED, __HIP_MEMORY_SCOPE_AGENT) + TAG;
    const unsigned em = __builtin_amdgcn_readlane(e, hl);
    wait_bar<63>(); wait_bar<0>();
    if (!iw) {
      if (w == 0 && lane < 4) __hip_atomic_store(flags + peer * 288 + hl * 32, e, __ATOMIC_RELAXED, __HIP_MEMORY_SCOPE_AGENT);
      while (__any(__hip_atomic_load(mine, __ATOMIC_RELAXED, __HIP_MEMORY_SCOPE_AGENT) != em)) __builtin_amdgcn_s_sleep(1);
      asm volatile("" ::: "memory");
    }
    issue(4); issue(5);
#pragma clang loop unroll(disable)
    for (int t = 6; t < 16; ++t) { wait_bar<18>(); issue(t); }
    wait_bar<18>(); wait_bar<9>(); wait_bar<0>(); wait_bar<0>();
    asm volatile("s_barrier" ::: "memory");
    if (w == 0) {
      if ((lane >> 2) == 1) __hip_atomic_store(flags + peer * 288 + (4 + kq) * 32, e, __ATOMIC_RELAXED, __HIP_MEMORY_SCOPE_AGENT);
      while (__any(__hip_atomic_load(mine + 128, __ATOMIC_RELAXED, __HIP_MEMORY_SCOPE_AGENT) != em)) __builtin_amdgcn_s_sleep(1);
      asm volatile("s_barrier" ::: "memory");
      if (lane == 0) __hip_atomic_store(mine + 256, em, __ATOMIC_RELAXED, __HIP_MEMORY_SCOPE_AGENT);
    } else asm volatile("s_barrier" ::: "memory");
    return;
  }
  const rsrc_t rs = mk_rsrc(xch + (size_t)(blockIdx.x >> 2) * 176 * 1024, 176 * 1024);
  u32x2* po = out + (b * SQ * 2 * DM + br * DM + hl * 144) / 4;
  if (w == 0) oproj_body<0, 0, 5, 0, 4, true>(lx, lane, kq, rs, po);
  else if (w == 1) oproj_body<1, 0, 4, 4, 5, false>(lx, lane, kq, rs, po);
  else if (w == 2) oproj_body<2, 4, 5, 5, 4, false>(lx, lane, kq, rs, po);
  else oproj_body<3, 5, 4, 0, 5, false>(lx, lane, kq, rs, po);
}

extern "C" void kernel_launch(void* const* d_in, const int* in_sizes, int n_in, void* d_out, int out_size, void* d_ws, size_t ws_size, hipStream_t stream) {
  Ptrs pa = {d_in[0], d_in[1], d_in[2], d_in[3], d_in[4]}, pb = {d_in[5], d_in[6], d_in[7], d_in[8], d_in[9]};
  char* ws = (char*)d_ws;
  fused_kernel<<<256, 512, 0, stream>>>(pa, pb, ws, ws + (32u << 20), (unsigned*)(ws + (64u << 20)), (u32x2*)d_out);
}
```
